```python
import torch

def _softmax(x, axis=-1):
    return torch.softmax(x, dim=axis)
def _gelu(x):
    return torch.nn.functional.gelu(x, approximate="tanh")
def _silu(x):
    return torch.nn.functional.silu(x)
def _sigmoid(x):
    return torch.sigmoid(x)
def _layernorm(x, axis=-1, eps=1e-5):
    m = x.mean(dim=axis, keepdim=True)
    v = x.var(dim=axis, keepdim=True, unbiased=False)
    return (x - m) / torch.sqrt(v + eps)
def _scatter_add(tgt, idx, src, axis=0):
    return tgt.clone().index_add_(axis, idx, src)
def _segment_sum(src, seg, n):
    out = torch.zeros((n,) + src.shape[1:], dtype=src.dtype)
    return out.index_add_(0, seg, src)
def _pad(x, pw):
    flat = tuple(v for pair in reversed(pw) for v in pair)
    return torch.nn.functional.pad(x, flat)

kernel_name = 'gated_mix__sliding_block_cached__glu_gated_geglu__decode'

def setup_inputs():

    _w = (lambda a: (lambda t: t.to(torch.bfloat16) if t.is_floating_point() else t)(torch.as_tensor(a)))
    def _A_setup():
        B, Sq, D = 1, 1, 640
        Hq, Hkv, Sk, Dh = 32, 32, 4096, 192
        assert Hq % Hkv == 0, (Hq, Hkv)
        return {
            "x": _w((lambda *s: torch.randn(*s, dtype=torch.bfloat16) * 0.05)(B, Sq, D)),
            "k_cache": _w((lambda *s: torch.randn(*s, dtype=torch.bfloat16) * 6.4)(B, Hkv, Sk, Dh)),
            "v_cache": _w((lambda *s: torch.randn(*s, dtype=torch.bfloat16) * 0.05)(B, Hkv, Sk, Dh)),
            "wq": _w((lambda *s: torch.randn(*s, dtype=torch.bfloat16) * 6.4)(D, Hq * Dh) * (D ** -0.5)),
            "wo": _w((lambda *s: torch.randn(*s, dtype=torch.bfloat16) * 0.05)(Hq * Dh, D) * ((Hq * Dh) ** -0.5)),
        }
    _A_in = _A_setup()

    def _B_setup():
        B, S, Dm, Df = 1, 1, 640, 1536
        return {
            "x": _w((lambda *s: torch.randn(*s, dtype=torch.bfloat16) * 0.05)(B, S, Dm)),
            "w_gate": _w((lambda *s: torch.randn(*s, dtype=torch.bfloat16) * 0.8)(Dm, Df) * (Dm ** -0.5)),
            "w_up": _w((lambda *s: torch.randn(*s, dtype=torch.bfloat16) * 0.8)(Dm, Df) * (Dm ** -0.5)),
            "w_down": _w((lambda *s: torch.randn(*s, dtype=torch.bfloat16) * 0.8)(Df, Dm) * (Df ** -0.5)),
        }
    _B_in = _B_setup()

    return {
        "A_x": _A_in["x"],
        "A_k_cache": _A_in["k_cache"],
        "A_v_cache": _A_in["v_cache"],
        "A_wq": _A_in["wq"],
        "A_wo": _A_in["wo"],
        "B_x": _B_in["x"],
        "B_w_gate": _B_in["w_gate"],
        "B_w_up": _B_in["w_up"],
        "B_w_down": _B_in["w_down"],
        "gate_w": _w((lambda *s: torch.randn(*s, dtype=torch.bfloat16) * 0.05)(640) * 16.0),
    }

def reference(A_x, A_k_cache, A_v_cache, A_wq, A_wo, B_x, B_w_gate, B_w_up, B_w_down, gate_w):
    def _A_ref(x, k_cache, v_cache, wq, wo):
        B, Sq, D = x.shape
        Hq, Dh = 32, 192
        Hkv, Sk = k_cache.shape[1], k_cache.shape[2]
        W = min(4096, max(256, Sk // 4))
        rep = Hq // Hkv
        q = (lambda x, *d: x.permute(*d))((x @ wq).reshape(B, Sq, Hq, Dh), 0, 2, 1, 3)
        k = (lambda x, n, axis: torch.repeat_interleave(x, n, dim=axis))(k_cache[:, :, Sk - W:, :], rep, axis=1)
        v = (lambda x, n, axis: torch.repeat_interleave(x, n, dim=axis))(v_cache[:, :, Sk - W:, :], rep, axis=1)
        scores = torch.einsum("bhqd,bhkd->bhqk", q, k) * (Dh ** -0.5)
        p = _softmax(scores, axis=-1)
        o = torch.einsum("bhqk,bhkd->bhqd", p, v)
        return (lambda x, *d: x.permute(*d))(o, 0, 2, 1, 3).reshape(B, Sq, Hq * Dh) @ wo

    def _B_ref(x, w_gate, w_up, w_down):
        g = torch.einsum("bsd,df->bsf", x, w_gate)
        u = torch.einsum("bsd,df->bsf", x, w_up)
        h = _gelu(g) * u
        return torch.einsum("bsf,fd->bsd", h, w_down)

    x = A_x
    g = _sigmoid((x * gate_w).sum(-1, keepdims=True))
    a = _A_ref(x, A_k_cache, A_v_cache, A_wq, A_wo)
    b = _B_ref(x, B_w_gate, B_w_up, B_w_down)
    return g * a + (1 - g) * b
```

```cpp
#include <hip/hip_runtime.h>
#include <hip/hip_bf16.h>
#include <stdint.h>

typedef unsigned short u16;
__device__ __forceinline__ float bf2f(u16 v) { return __uint_as_float(((unsigned)v) << 16); }
__device__ __forceinline__ u16 f2bf(float f) { __hip_bfloat16 b = __float2bfloat16(f); return *(u16*)&b; }
__device__ __forceinline__ float rbf(float f) { return bf2f(f2bf(f)); }

constexpr int D = 640, H = 32, SK = 4096, DH = 192, W = 1024, DF = 1536, HD = H * DH;

__global__ void k1(const u16* x, const u16* wq, const u16* wg, const u16* wu, float* q, float* h) {
  int c = blockIdx.x * 64 + (threadIdx.x & 63);
  int part = threadIdx.x >> 6;
  __shared__ float red[2][4][64];
  if (c < HD) {
    float a = 0;
    for (int k = part; k < D; k += 4) a += bf2f(x[k]) * bf2f(wq[(size_t)k * HD + c]);
    red[0][part][threadIdx.x & 63] = a;
    __syncthreads();
    if (part == 0) q[c] = rbf(red[0][0][threadIdx.x] + red[0][1][threadIdx.x] + red[0][2][threadIdx.x] + red[0][3][threadIdx.x]);
  } else {
    c -= HD;
    float a = 0, b = 0;
    for (int k = part; k < D; k += 4) { a += bf2f(x[k]) * bf2f(wg[(size_t)k * DF + c]); b += bf2f(x[k]) * bf2f(wu[(size_t)k * DF + c]); }
    red[0][part][threadIdx.x & 63] = a; red[1][part][threadIdx.x & 63] = b;
    __syncthreads();
    if (part == 0) {
      float g = rbf(red[0][0][threadIdx.x] + red[0][1][threadIdx.x] + red[0][2][threadIdx.x] + red[0][3][threadIdx.x]);
      float u = rbf(red[1][0][threadIdx.x] + red[1][1][threadIdx.x] + red[1][2][threadIdx.x] + red[1][3][threadIdx.x]);
      float t = 0.7978845608028654f * (g + 0.044715f * g * g * g);
      float ge = rbf(0.5f * g * (1.f + tanhf(t)));
      h[c] = rbf(ge * u);
    }
  }
}

__global__ void k2(const float* q, const u16* kc, const u16* vc, float* o) {
  int h = blockIdx.x;
  __shared__ float s[W];
  __shared__ float red[1024];
  __shared__ float qs[DH];
  int t = threadIdx.x;
  if (t < DH) qs[t] = q[h * DH + t];
  __syncthreads();
  const u16* kp = kc + ((size_t)h * SK + (SK - W) + t) * DH;
  float a = 0;
  for (int d = 0; d < DH; d++) a += qs[d] * bf2f(kp[d]);
  a = rbf(rbf(a) * rbf(0.07216878364870322f));
  red[t] = a; __syncthreads();
  for (int o2 = 512; o2 > 0; o2 >>= 1) { if (t < o2) red[t] = fmaxf(red[t], red[t + o2]); __syncthreads(); }
  float m = red[0]; __syncthreads();
  float e = expf(a - m);
  red[t] = e; __syncthreads();
  for (int o2 = 512; o2 > 0; o2 >>= 1) { if (t < o2) red[t] += red[t + o2]; __syncthreads(); }
  float l = red[0]; __syncthreads();
  s[t] = rbf(e / l);
  __syncthreads();
  int d = t % DH, part = t / DH;
  float acc = 0;
  if (part < 5) for (int k = part; k < W; k += 5) acc += s[k] * bf2f(vc[((size_t)h * SK + (SK - W) + k) * DH + d]);
  red[t] = acc; __syncthreads();
  if (t < DH) o[h * DH + t] = rbf(red[t] + red[t + DH] + red[t + 2 * DH] + red[t + 3 * DH] + red[t + 4 * DH]);
}

__global__ void k3(const u16* x, const u16* gw, const float* o, const float* hh, const u16* wo, const u16* wd, u16* out) {
  __shared__ float red[2][16][64];
  __shared__ float gr[1024];
  int t = threadIdx.x;
  int c = blockIdx.x * 64 + (t & 63), part = t >> 6;
  float a = 0, b = 0;
  for (int k = part; k < HD; k += 16) a += o[k] * bf2f(wo[(size_t)k * D + c]);
  for (int k = part; k < DF; k += 16) b += hh[k] * bf2f(wd[(size_t)k * D + c]);
  red[0][part][t & 63] = a; red[1][part][t & 63] = b;
  gr[t] = t < D ? rbf(bf2f(x[t]) * bf2f(gw[t])) : 0.f;
  __syncthreads();
  for (int o2 = 512; o2 > 0; o2 >>= 1) { if (t < o2) gr[t] += gr[t + o2]; __syncthreads(); }
  if (part == 0) {
    float sa = 0, sb = 0;
    for (int i = 0; i < 16; i++) { sa += red[0][i][t]; sb += red[1][i][t]; }
    sa = rbf(sa); sb = rbf(sb);
    float z = rbf(gr[0]);
    float g = rbf(1.f / (1.f + expf(-z)));
    float r = rbf(rbf(g * sa) + rbf(rbf(1.f - g) * sb));
    out[c] = f2bf(r);
  }
}

extern "C" void kernel_launch(void* const* d_in, const int* in_sizes, int n_in,
                              void* d_out, int out_size, void* d_ws, size_t ws_size,
                              hipStream_t stream) {
  const u16* x = (const u16*)d_in[0];
  const u16* kc = (const u16*)d_in[1];
  const u16* vc = (const u16*)d_in[2];
  const u16* wq = (const u16*)d_in[3];
  const u16* wo = (const u16*)d_in[4];
  const u16* wg = (const u16*)d_in[6];
  const u16* wu = (const u16*)d_in[7];
  const u16* wd = (const u16*)d_in[8];
  const u16* gw = (const u16*)d_in[9];
  float* q = (float*)d_ws;
  float* h = q + HD;
  float* o = h + DF;
  k1<<<(HD + DF) / 64, 256, 0, stream>>>(x, wq, wg, wu, q, h);
  k2<<<H, 1024, 0, stream>>>(q, kc, vc, o);
  k3<<<D / 64, 1024, 0, stream>>>(x, gw, o, h, wo, wd, (u16*)d_out);
}
```

```cpp
#include <hip/hip_runtime.h>
#include <hip/hip_bf16.h>
#include <stdint.h>

namespace {

typedef unsigned u32;
typedef unsigned long long u64;
typedef unsigned v4u __attribute__((ext_vector_type(4)));
typedef unsigned v2u __attribute__((ext_vector_type(2)));
typedef float f32x4 __attribute__((ext_vector_type(4)));
typedef short bf16x8 __attribute__((ext_vector_type(8)));
typedef __bf16 bf16x2 __attribute__((ext_vector_type(2)));
#define GAS __attribute__((address_space(1)))

constexpr int DM = 640, NH = 32, SK = 4096, DH = 192, WIN = 1024, DF = 1536, HD = NH * DH;

constexpr u32 WS_EPOCH = 0;
constexpr u32 WS_X1Q = 4096;
constexpr u32 WS_X1M = WS_X1Q + 32 * 8 * 192 * 8;
constexpr u32 WS_X2 = WS_X1M + 8 * 10 * 192 * 8;
constexpr u32 WS_X3 = WS_X2 + 32 * 8 * 256 * 8;
constexpr u32 WS_END = WS_X3 + 2 * 128 * 320 * 8;
constexpr u32 WS_STAMP = 4u << 20;

constexpr u32 SPIN_CAP = 1u << 16;

#define ND1 3
#define G2_SLEEP 8

template <int CTRL> __device__ __forceinline__ float dpp(float x) {
  return __builtin_bit_cast(float, __builtin_amdgcn_mov_dpp(__builtin_bit_cast(int, x), CTRL, 0xf, 0xf, true));
}
constexpr int XOR1 = 0xB1, XOR2 = 0x4E, HMIR = 0x141, ROR8 = 0x128, ROR4 = 0x124;
__device__ __forceinline__ float swap16_max(float x) {
  auto s = __builtin_amdgcn_permlane16_swap(__float_as_uint(x), __float_as_uint(x), false, false);
  return fmaxf(__uint_as_float(s[0]), __uint_as_float(s[1]));
}
__device__ __forceinline__ float swap32_max(float x) {
  auto s = __builtin_amdgcn_permlane32_swap(__float_as_uint(x), __float_as_uint(x), false, false);
  return fmaxf(__uint_as_float(s[0]), __uint_as_float(s[1]));
}
__device__ __forceinline__ float swap16_sum(float x) {
  auto s = __builtin_amdgcn_permlane16_swap(__float_as_uint(x), __float_as_uint(x), false, false);
  return __uint_as_float(s[0]) + __uint_as_float(s[1]);
}
__device__ __forceinline__ float swap32_sum(float x) {
  auto s = __builtin_amdgcn_permlane32_swap(__float_as_uint(x), __float_as_uint(x), false, false);
  return __uint_as_float(s[0]) + __uint_as_float(s[1]);
}
__device__ __forceinline__ u32 pk_bf16(float lo, float hi) {
  bf16x2 z = {(__bf16)lo, (__bf16)hi};
  return __builtin_bit_cast(u32, z);
}
__device__ __forceinline__ float dot2(u32 a, u32 b, float c) {
  return __builtin_amdgcn_fdot2_f32_bf16(__builtin_bit_cast(bf16x2, a), __builtin_bit_cast(bf16x2, b), c, false);
}
__device__ __forceinline__ f32x4 sel_mfma(u32 cdup, const v4u& msk, const v4u& tile, f32x4 acc) {
  v4u a = {cdup & msk.x, cdup & msk.y, cdup & msk.z, cdup & msk.w};
  return __builtin_amdgcn_mfma_f32_16x16x32_bf16(__builtin_bit_cast(bf16x8, a), __builtin_bit_cast(bf16x8, tile), acc, 0, 0, 0);
}
__device__ __forceinline__ void st_granule(const char* base, u32 off, float v, u32 tag) {
  __hip_atomic_store((GAS u64*)(base + off), ((u64)tag << 32) | __float_as_uint(v), __ATOMIC_RELAXED, __HIP_MEMORY_SCOPE_AGENT);
}
#define BAR() asm volatile("s_waitcnt lgkmcnt(0)\n\ts_barrier" ::: "memory")
#define LDT(dst, voff, base, imm) asm volatile("global_load_dwordx4 %0, %1, %2 offset:" #imm " nt" : "=v"(dst) : "v"(voff), "s"(base))

#define STAMP(n) do { } while (0)

struct Lds {
  float pa[8 * 2 * 64];
  float po[8 * 2 * 192];
  float ml[16];
  float zp[8];
  u32 cd[64];
  u32 qs[96];
};

__global__ void __launch_bounds__(512, 2)
fused_decode_kernel(const char* __restrict__ x, const char* __restrict__ kc, const char* __restrict__ vc,
                    const char* __restrict__ wq, const char* __restrict__ wo, const char* __restrict__ wg,
                    const char* __restrict__ wu, const char* __restrict__ wd, const char* __restrict__ gw,
                    char* __restrict__ out, char* __restrict__ ws) {
  __shared__ __attribute__((aligned(16))) Lds lds;
  STAMP(0);
  const u32 tid = threadIdx.x, lane = tid & 63;
  const u32 w = __builtin_amdgcn_readfirstlane(tid >> 6);
  const u32 b = blockIdx.x, Q = b & 7, i = b >> 3, h = 4 * Q + (i & 3), s = i >> 2, q4 = s >> 1, half = s & 1;
  const u32 kr = lane >> 3, ch = lane & 7;

  const u32 lc4 = w & 3, hf = w >> 2, isM = (lc4 == 3);
  const u32 im = i < 30 ? i : 29, lc6 = im % 6, rs = im / 6, mat = lc6 / 3, lcx = lc6 % 3;
  const u32 xrow0 = isM ? 128 * rs + 64 * hf : 80 * s + 40 * hf;
  const u32 RS = isM ? DF * 2 : HD * 2;
  const char* abase = isM ? (mat ? wu : wg) + (size_t)xrow0 * (DF * 2) + (3 * Q + lcx) * 128
                          : wq + (size_t)xrow0 * (HD * 2) + (3 * h + lc4) * 128;
  u32 xs, ep, xz, gz;
  {
    const u32 o1 = min(xrow0 / 2 + lane, 319u) * 4, o2 = (40 * w + min(lane, 39u)) * 4, z0 = 0;
    asm volatile("global_load_dword %0, %1, %2" : "=v"(xs) : "v"(o1), "s"(x));
    asm volatile("global_load_dword %0, %1, %2 sc1" : "=v"(ep) : "v"(z0), "s"(ws));
    asm volatile("global_load_dword %0, %1, %2" : "=v"(xz) : "v"(o2), "s"(x));
    asm volatile("global_load_dword %0, %1, %2" : "=v"(gz) : "v"(o2), "s"(gw));
  }
  v4u ta[8];
  {
    const u32 va = kr * RS + ch * 16, ts = 8 * RS;
    LDT(ta[0], va, abase, 0);
    LDT(ta[1], va + ts, abase, 0);
    LDT(ta[2], va + 2 * ts, abase, 0);
    LDT(ta[3], va + 3 * ts, abase, 0);
    LDT(ta[4], va + 4 * ts, abase, 0);
    asm volatile("s_cmp_lg_u32 %7, 3\n\ts_cbranch_scc1 1f\n\t"
                 "global_load_dwordx4 %0, %3, %6 nt\n\tglobal_load_dwordx4 %1, %4, %6 nt\n\tglobal_load_dwordx4 %2, %5, %6 nt\n1:"
                 : "=&v"(ta[5]), "=&v"(ta[6]), "=&v"(ta[7])
                 : "v"(va + 5 * ts), "v"(va + 6 * ts), "v"(va + 7 * ts), "s"(abase), "s"(lc4) : "scc");
  }
  v4u tk[6], tv[6];
  const u32 vkv = kr * 384 + ch * 16;
  const size_t kvoff = ((size_t)h * SK + (SK - WIN) + 128 * s + 16 * w) * 384;
  const char* kbase = kc + kvoff;
  const char* vbase = vc + kvoff;
  LDT(tk[0], vkv, kbase, 0);
  LDT(tk[1], vkv, kbase, 128);
  LDT(tk[2], vkv, kbase, 256);
  LDT(tk[3], vkv, kbase, 3072);
  STAMP(1);

  const u32 hm = (ch & 1) ? 0xffff0000u : 0x0000ffffu;
  const v4u msk = {(ch >> 1) == 0 ? hm : 0u, (ch >> 1) == 1 ? hm : 0u, (ch >> 1) == 2 ? hm : 0u, (ch >> 1) == 3 ? hm : 0u};

  asm volatile("s_waitcnt vmcnt(12)\n\ts_cmp_eq_u32 %4, 3\n\ts_cbranch_scc1 1f\n\ts_waitcnt vmcnt(9)\n1:"
               : "+v"(xs), "+v"(ep), "+v"(xz), "+v"(gz) : "s"(lc4) : "scc");
  const u32 tag = __builtin_amdgcn_readfirstlane(ep) + 1u;
  u32 xd[8];
  {
    const u32 psel = (kr & 1) ? 0x03020302u : 0x01000100u;
#pragma unroll
    for (int G = 0; G < 8; G++) {
      const u32 d = __builtin_amdgcn_ds_bpermute((4 * G + (kr >> 1)) * 4, xs);
      xd[G] = __builtin_amdgcn_perm(d, d, psel);
    }
  }
  {
    float z = lane < 40 ? dot2(xz, gz, 0.f) : 0.f;
    z += dpp<XOR1>(z); z += dpp<XOR2>(z); z += dpp<HMIR>(z); z += dpp<ROR8>(z);
    z = swap32_sum(swap16_sum(z));
    if (lane == 0) lds.zp[w] = z;
  }
  STAMP(2);
  asm volatile("s_waitcnt vmcnt(4)"
               : "+v"(ta[0]), "+v"(ta[1]), "+v"(ta[2]), "+v"(ta[3]), "+v"(ta[4]), "+v"(ta[5]), "+v"(ta[6]), "+v"(ta[7]));
  STAMP(3);
  {
    f32x4 acc = {0.f, 0.f, 0.f, 0.f};
#pragma unroll
    for (int G = 0; G < 5; G++) acc = sel_mfma(xd[G], msk, ta[G], acc);
    if (isM) {
#pragma unroll
      for (int G = 5; G < 8; G++) acc = sel_mfma(xd[G], msk, ta[G], acc);
    }
    if ((lane >> 5) == ((lane >> 3) & 1))
      *(f32x4*)&lds.pa[(w * 2 + (lane >> 5)) * 64 + 8 * ch + 4 * ((lane >> 4) & 1)] = acc;
  }
  BAR();
  if (w < 4) {
    const float v = (lds.pa[(w * 2) * 64 + lane] + lds.pa[(w * 2 + 1) * 64 + lane]) +
                    (lds.pa[(w * 2 + 8) * 64 + lane] + lds.pa[(w * 2 + 9) * 64 + lane]);
    if (w < 3) st_granule(ws + WS_X1Q + (h * 8 + s) * 1536 + w * 512, lane * 8, v, tag);
    else if (i < 30) st_granule(ws + WS_X1M + (Q * 10 + mat * 5 + rs) * 1536 + lcx * 512, lane * 8, v, tag);
  }
  STAMP(4);
  LDT(tk[4], vkv, kbase, 3200);
  LDT(tk[5], vkv, kbase, 3328);
  LDT(tv[0], vkv, vbase, 0);
  LDT(tv[1], vkv, vbase, 128);
  LDT(tv[2], vkv, vbase, 256);
  const u32 kq = 4 * (h & 3) + q4;
  const char* g1base = (w < 3 ? ws + WS_X1Q + h * (8 * 1536) + w * 512 : ws + WS_X1M + Q * (10 * 1536)) + 3072;
  const u32 g1off = w < 3 ? lane * 8 : (12 * kq + min(lane, 11u)) * 8;
  const u32 g1off2 = g1off + 7680;
  v2u g1[10];
#define G1_LOADS()                                                                                                          \
  asm volatile("s_cmp_gt_u32 %13, 3\n\ts_cbranch_scc1 1f\n\t"                                                               \
               "global_load_dwordx2 %0, %10, %12 offset:-3072 sc1\n\tglobal_load_dwordx2 %1, %10, %12 offset:-1536 sc1\n\t" \
               "global_load_dwordx2 %2, %10, %12 sc1\n\tglobal_load_dwordx2 %3, %10, %12 offset:1536 sc1\n\t"               \
               "global_load_dwordx2 %4, %10, %12 offset:3072 sc1\n\tglobal_load_dwordx2 %5, %11, %12 offset:-3072 sc1\n\t"  \
               "global_load_dwordx2 %6, %11, %12 offset:-1536 sc1\n\tglobal_load_dwordx2 %7, %11, %12 sc1\n\t"              \
               "s_cmp_lg_u32 %13, 3\n\ts_cbranch_scc1 1f\n\t"                                                               \
               "global_load_dwordx2 %8, %11, %12 offset:1536 sc1\n\tglobal_load_dwordx2 %9, %11, %12 offset:3072 sc1\n1:"   \
               : "=&v"(g1[0]), "=&v"(g1[1]), "=&v"(g1[2]), "=&v"(g1[3]), "=&v"(g1[4]), "=&v"(g1[5]), "=&v"(g1[6]),          \
                 "=&v"(g1[7]), "=&v"(g1[8]), "=&v"(g1[9])                                                                   \
               : "v"(g1off), "v"(g1off2), "s"(g1base), "s"(w) : "scc")
  G1_LOADS();
  LDT(tv[3], vkv, vbase, 3072);
  LDT(tv[4], vkv, vbase, 3200);
  LDT(tv[5], vkv, vbase, 3328);
  const u32 isD = w >= 3;
  const u32 line = 5 * half + (isD ? w - 3 : 0);
  const char* obase = wo + (size_t)(DH * h + 48 * q4) * 1280 + line * 128;
  const char* dbase = wd + (size_t)(DH * Q + 12 * kq) * 1280 + line * 128;
  const u32 vd = kr * 1280 + ch * 16;
  v4u td[8];
#define LDT_IF_D(dst, voff, base)                                                                \
  asm volatile("s_cmp_lt_u32 %3, 3\n\ts_cbranch_scc1 1f\n\tglobal_load_dwordx4 %0, %1, %2 nt\n1:" \
               : "=v"(dst) : "v"(voff), "s"(base), "s"(w) : "scc")
#pragma unroll
  for (int G = 0; G < ND1; G++) LDT_IF_D(td[G], vd + G * 10240, obase);
  STAMP(5);

  asm volatile("s_waitcnt vmcnt(%c19)\n\ts_cmp_ge_u32 %18, 3\n\ts_cbranch_scc1 1f\n\ts_waitcnt vmcnt(3)\n1:"
               : "+v"(tk[0]), "+v"(tk[1]), "+v"(tk[2]), "+v"(tk[3]), "+v"(tk[4]), "+v"(tk[5]), "+v"(tv[0]), "+v"(tv[1]), "+v"(tv[2]),
                 "+v"(g1[0]), "+v"(g1[1]), "+v"(g1[2]), "+v"(g1[3]), "+v"(g1[4]), "+v"(g1[5]), "+v"(g1[6]), "+v"(g1[7]), "+v"(g1[8])
               : "s"(w), "i"(3 + ND1) : "scc");
  asm volatile("" : "+v"(g1[9]));
  STAMP(6);
  if (w < 4) {
    const int n = w < 3 ? 8 : 10;
    for (u32 spin = 0; spin < SPIN_CAP; spin++) {
      bool ok = g1[0].y == tag && g1[1].y == tag && g1[2].y == tag && g1[3].y == tag && g1[4].y == tag && g1[5].y == tag &&
                g1[6].y == tag && g1[7].y == tag;
      if (n == 10) ok = ok && g1[8].y == tag && g1[9].y == tag;
      if (__all(ok)) break;
      G1_LOADS();
      asm volatile("s_waitcnt vmcnt(0)"
                   : "+v"(g1[0]), "+v"(g1[1]), "+v"(g1[2]), "+v"(g1[3]), "+v"(g1[4]), "+v"(g1[5]), "+v"(g1[6]), "+v"(g1[7]),
                     "+v"(g1[8]), "+v"(g1[9]));
    }
#define F(j) __uint_as_float(g1[j].x)
    if (w < 3) {
      const float qv = ((F(0) + F(1)) + (F(2) + F(3))) + ((F(4) + F(5)) + (F(6) + F(7)));
      ((unsigned short*)lds.qs)[w * 64 + lane] = (unsigned short)pk_bf16(qv, qv);
    } else {
      const float g = ((F(0) + F(1)) + (F(2) + F(3))) + F(4), u = ((F(5) + F(6)) + (F(7) + F(8))) + F(9);
      const float z = ((lds.zp[0] + lds.zp[1]) + (lds.zp[2] + lds.zp[3])) + ((lds.zp[4] + lds.zp[5]) + (lds.zp[6] + lds.zp[7]));
      const float omg = 1.f - __builtin_amdgcn_rcpf(1.f + __builtin_amdgcn_exp2f(-1.4426950408889634f * z));
      const float y2 = 2.f * 0.7978845608028654f * 1.4426950408889634f * (g + 0.044715f * g * g * g);
      const float ge = g - g * __builtin_amdgcn_rcpf(1.f + __builtin_amdgcn_exp2f(y2));
      const float hv = lane < 12 ? ge * u * omg : 0.f;
      if (lane < 16) lds.cd[(lane & 7) * 8 + 6 + (lane >> 3)] = pk_bf16(hv, hv);
    }
#undef F
  }
  BAR();
  STAMP(7);
  f32x4 ao[3];
  float mw, lw;
  {
    const v4u q0 = *(const v4u*)&lds.qs[4 * ch], q1 = *(const v4u*)&lds.qs[32 + 4 * ch], q2 = *(const v4u*)&lds.qs[64 + 4 * ch];
    float s0 = 0.f, s1 = 0.f;
#pragma unroll
    for (int e = 0; e < 4; e++) { s0 = dot2(tk[0][e], q0[e], s0); s1 = dot2(tk[3][e], q0[e], s1); }
#pragma unroll
    for (int e = 0; e < 4; e++) { s0 = dot2(tk[1][e], q1[e], s0); s1 = dot2(tk[4][e], q1[e], s1); }
#pragma unroll
    for (int e = 0; e < 4; e++) { s0 = dot2(tk[2][e], q2[e], s0); s1 = dot2(tk[5][e], q2[e], s1); }
    s0 += dpp<XOR1>(s0); s1 += dpp<XOR1>(s1);
    s0 += dpp<XOR2>(s0); s1 += dpp<XOR2>(s1);
    s0 += dpp<HMIR>(s0); s1 += dpp<HMIR>(s1);
    constexpr float C2 = 0.07216878364870322f * 1.4426950408889634f;
    s0 *= C2; s1 *= C2;
    float m = fmaxf(s0, s1);
    m = fmaxf(m, dpp<ROR8>(m));
    m = swap32_max(swap16_max(m));
    const float p0 = __builtin_amdgcn_exp2f(s0 - m), p1 = __builtin_amdgcn_exp2f(s1 - m);
    const u32 pd0 = pk_bf16(p0, p0), pd1 = pk_bf16(p1, p1);
    float l = __uint_as_float(pd0 & 0xffff0000u) + __uint_as_float(pd1 & 0xffff0000u);
    l += dpp<ROR8>(l);
    l = swap32_sum(swap16_sum(l));
    mw = m; lw = l;
    const f32x4 zero = {0.f, 0.f, 0.f, 0.f};
#pragma unroll
    for (int e = 0; e < 3; e++) ao[e] = sel_mfma(pd0, msk, tv[e], zero);
    STAMP(8);
    asm volatile("s_waitcnt vmcnt(%c4)\n\ts_cmp_ge_u32 %3, 3\n\ts_cbranch_scc1 1f\n\ts_waitcnt vmcnt(0)\n1:"
                 : "+v"(tv[3]), "+v"(tv[4]), "+v"(tv[5]) : "s"(w), "i"(ND1) : "scc");
#pragma unroll
    for (int G = ND1; G < 6; G++) LDT_IF_D(td[G], vd + G * 10240, obase);
    LDT_IF_D(td[6], vd, dbase);
    LDT_IF_D(td[7], (kr & 3) * 1280 + ch * 16 + 10240, dbase);
#pragma unroll
    for (int e = 0; e < 3; e++) ao[e] = sel_mfma(pd1, msk, tv[3 + e], ao[e]);
  }
  if ((lane >> 5) == ((lane >> 3) & 1)) {
    float* p = &lds.po[(w * 2 + (lane >> 5)) * 192 + 8 * ch + 4 * ((lane >> 4) & 1)];
    *(f32x4*)p = ao[0];
    *(f32x4*)(p + 64) = ao[1];
    *(f32x4*)(p + 128) = ao[2];
  }
  if (lane == 0) { lds.ml[w] = mw; lds.ml[8 + w] = lw; }
  BAR();
  STAMP(9);
  if (w < 4) {
    const f32x4 m0 = *(const f32x4*)&lds.ml[0], m1 = *(const f32x4*)&lds.ml[4];
    const f32x4 l0 = *(const f32x4*)&lds.ml[8], l1 = *(const f32x4*)&lds.ml[12];
    const float M = fmaxf(fmaxf(fmaxf(m0.x, m0.y), fmaxf(m0.z, m0.w)), fmaxf(fmaxf(m1.x, m1.y), fmaxf(m1.z, m1.w)));
    float f[8];
#pragma unroll
    for (int e = 0; e < 4; e++) { f[e] = __builtin_amdgcn_exp2f(m0[e] - M); f[4 + e] = __builtin_amdgcn_exp2f(m1[e] - M); }
    float v;
    if (w < 3) {
      const float* p = &lds.po[w * 64 + lane];
      v = 0.f;
#pragma unroll
      for (int e = 0; e < 8; e++) v = fmaf(f[e], p[(2 * e) * 192] + p[(2 * e + 1) * 192], v);
    } else {
      float L = 0.f;
#pragma unroll
      for (int e = 0; e < 4; e++) L = fmaf(f[e], l0[e], fmaf(f[4 + e], l1[e], L));
      v = lane == 0 ? M : L;
    }
    if (w < 3 || lane < 2) st_granule(ws + WS_X2 + (h * 8 + s) * 2048 + w * 512, lane * 8, v, tag);
  }
  STAMP(10);
  if (w == 0) {
    const char* g2base = ws + WS_X2 + h * (8 * 2048);
    const u32 go = (lane < 48 ? 48 * q4 + lane : 192 + (lane & 1)) * 8;
    const u32 gm = (lane & 7) * 2048 + (192 + ((lane >> 3) & 1)) * 8;
    v2u g2[9];
    __builtin_amdgcn_s_sleep(G2_SLEEP);
    for (u32 spin = 0; spin < SPIN_CAP; spin++) {
      asm volatile("global_load_dwordx2 %0, %9, %12 sc1\n\tglobal_load_dwordx2 %1, %9, %12 offset:2048 sc1\n\t"
                   "global_load_dwordx2 %2, %10, %12 offset:-4096 sc1\n\tglobal_load_dwordx2 %3, %10, %12 offset:-2048 sc1\n\t"
                   "global_load_dwordx2 %4, %10, %12 sc1\n\tglobal_load_dwordx2 %5, %10, %12 offset:2048 sc1\n\t"
                   "global_load_dwordx2 %6, %11, %12 offset:-2048 sc1\n\tglobal_load_dwordx2 %7, %11, %12 sc1\n\t"
                   "global_load_dwordx2 %8, %13, %12 sc1\n\ts_waitcnt vmcnt(0)"
                   : "=&v"(g2[0]), "=&v"(g2[1]), "=&v"(g2[2]), "=&v"(g2[3]), "=&v"(g2[4]), "=&v"(g2[5]), "=&v"(g2[6]),
                     "=&v"(g2[7]), "=&v"(g2[8])
                   : "v"(go), "v"(go + 4 * 2048), "v"(go + 7 * 2048), "s"(g2base), "v"(gm));
      const bool ok = g2[0].y == tag && g2[1].y == tag && g2[2].y == tag && g2[3].y == tag && g2[4].y == tag &&
                      g2[5].y == tag && g2[6].y == tag && g2[7].y == tag && g2[8].y == tag;
      if (__all(ok)) break;
    }
    STAMP(11);
    const float ms = __uint_as_float(g2[8].x);
    float M = ms;
    M = fmaxf(M, dpp<XOR1>(M)); M = fmaxf(M, dpp<XOR2>(M)); M = fmaxf(M, dpp<HMIR>(M));
    const float fs = __builtin_amdgcn_exp2f(ms - M);
    float Lp = fs * dpp<ROR8>(ms);
    Lp += dpp<XOR1>(Lp); Lp += dpp<XOR2>(Lp); Lp += dpp<HMIR>(Lp);
    const float z = ((lds.zp[0] + lds.zp[1]) + (lds.zp[2] + lds.zp[3])) + ((lds.zp[4] + lds.zp[5]) + (lds.zp[6] + lds.zp[7]));
    const float gate = __builtin_amdgcn_rcpf(1.f + __builtin_amdgcn_exp2f(-1.4426950408889634f * z));
    const float cs = fs * gate * __builtin_amdgcn_rcpf(Lp);
    float o = 0.f;
#pragma unroll
    for (int e = 0; e < 8; e++)
      o = fmaf(__builtin_bit_cast(float, __builtin_amdgcn_readlane(__builtin_bit_cast(int, cs), e)), __uint_as_float(g2[e].x), o);
    if (lane < 48) lds.cd[(lane & 7) * 8 + (lane >> 3)] = pk_bf16(o, o);
  }
  asm volatile("s_waitcnt vmcnt(0)"
               : "+v"(td[0]), "+v"(td[1]), "+v"(td[2]), "+v"(td[3]), "+v"(td[4]), "+v"(td[5]), "+v"(td[6]), "+v"(td[7]));
  BAR();
  STAMP(12);
  if (isD) {
    const v4u c0 = *(const v4u*)&lds.cd[kr * 8], c1 = *(const v4u*)&lds.cd[kr * 8 + 4];
    f32x4 acc = {0.f, 0.f, 0.f, 0.f};
#pragma unroll
    for (int G = 0; G < 4; G++) acc = sel_mfma(c0[G], msk, td[G], acc);
#pragma unroll
    for (int G = 0; G < 4; G++) acc = sel_mfma(c1[G], msk, td[4 + G], acc);
    float r[4];
#pragma unroll
    for (int e = 0; e < 4; e++) {
      const u32 u = __float_as_uint(dpp<ROR8>(acc[e]));
      auto sw = __builtin_amdgcn_permlane32_swap(u, u, false, false);
      r[e] = acc[e] + __uint_as_float(sw[1]);
    }
    if (lane < 32 && !(lane & 8)) {
      const char* x3 = ws + WS_X3 + ((half * 128 + h * 4 + q4) * 320 + (w - 3) * 64) * 8;
      const u32 off = (8 * ch + 4 * (lane >> 4)) * 8;
      const v4u a = {__float_as_uint(r[0]), tag, __float_as_uint(r[1]), tag};
      const v4u c = {__float_as_uint(r[2]), tag, __float_as_uint(r[3]), tag};
      asm volatile("global_store_dwordx4 %0, %1, %3 sc1\n\tglobal_store_dwordx4 %0, %2, %3 offset:16 sc1\n\ts_nop 1"
                   :: "v"(off), "v"(a), "v"(c), "s"(x3) : "memory");
    }
    STAMP(13);
  } else if (w == 0 && b < 80) {
    const u32 hr = b / 40, rr = b % 40;
    const char* x3 = ws + WS_X3 + (hr * 128 * 320 + 8 * rr) * 8;
    const u32 o0 = (lane >> 2) * 2560 + (lane & 3) * 16;
    v4u g3[8];
    for (u32 spin = 0; spin < SPIN_CAP; spin++) {
      asm volatile("global_load_dwordx4 %0, %8, %16 sc1\n\tglobal_load_dwordx4 %1, %9, %16 sc1\n\t"
                   "global_load_dwordx4 %2, %10, %16 sc1\n\tglobal_load_dwordx4 %3, %11, %16 sc1\n\t"
                   "global_load_dwordx4 %4, %12, %16 sc1\n\tglobal_load_dwordx4 %5, %13, %16 sc1\n\t"
                   "global_load_dwordx4 %6, %14, %16 sc1\n\tglobal_load_dwordx4 %7, %15, %16 sc1\n\ts_waitcnt vmcnt(0)"
                   : "=&v"(g3[0]), "=&v"(g3[1]), "=&v"(g3[2]), "=&v"(g3[3]), "=&v"(g3[4]), "=&v"(g3[5]), "=&v"(g3[6]), "=&v"(g3[7])
                   : "v"(o0), "v"(o0 + 40960), "v"(o0 + 2 * 40960), "v"(o0 + 3 * 40960), "v"(o0 + 4 * 40960),
                     "v"(o0 + 5 * 40960), "v"(o0 + 6 * 40960), "v"(o0 + 7 * 40960), "s"(x3));
      bool ok = true;
#pragma unroll
      for (int e = 0; e < 8; e++) ok = ok && g3[e].y == tag && g3[e].w == tag;
      if (__all(ok)) break;
    }
    STAMP(13);
    float a0 = 0.f, a1 = 0.f;
#pragma unroll
    for (int e = 0; e < 8; e++) { a0 += __uint_as_float(g3[e].x); a1 += __uint_as_float(g3[e].z); }
    a0 += dpp<ROR4>(a0); a1 += dpp<ROR4>(a1);
    a0 += dpp<ROR8>(a0); a1 += dpp<ROR8>(a1);
    a0 = swap32_sum(swap16_sum(a0)); a1 = swap32_sum(swap16_sum(a1));
    if (lane < 4) *(GAS u32*)(out + (8 * b + 2 * lane) * 2) = pk_bf16(a0, a1);
    if (b == 0 && lane == 0) __hip_atomic_store((GAS u32*)(ws + WS_EPOCH), tag, __ATOMIC_RELAXED, __HIP_MEMORY_SCOPE_AGENT);
  }
  STAMP(14);
}

}

extern "C" void kernel_launch(void* const* d_in, const int* in_sizes, int n_in, void* d_out, int out_size, void* d_ws,
                              size_t ws_size, hipStream_t stream) {
  fused_decode_kernel<<<256, 512, 0, stream>>>(
      (const char*)d_in[0], (const char*)d_in[1], (const char*)d_in[2], (const char*)d_in[3], (const char*)d_in[4],
      (const char*)d_in[6], (const char*)d_in[7], (const char*)d_in[8], (const char*)d_in[9], (char*)d_out, (char*)d_ws);
}
```

```cpp
#include <hip/hip_runtime.h>
#include <hip/hip_bf16.h>
#include <stdint.h>

namespace {

typedef unsigned u32;
typedef unsigned long long u64;
typedef unsigned v4u __attribute__((ext_vector_type(4)));
typedef unsigned v2u __attribute__((ext_vector_type(2)));
typedef float f32x4 __attribute__((ext_vector_type(4)));
typedef short bf16x8 __attribute__((ext_vector_type(8)));
typedef __bf16 bf16x2 __attribute__((ext_vector_type(2)));
#define GAS __attribute__((address_space(1)))

constexpr int DM = 640, NH = 32, SK = 4096, DH = 192, WIN = 1024, DF = 1536, HD = NH * DH;

constexpr u32 WS_EPOCH = 0;
constexpr u32 WS_X1Q = 4096;
constexpr u32 WS_X1M = WS_X1Q + 32 * 8 * 192 * 8;
constexpr u32 WS_X2 = WS_X1M + 8 * 10 * 192 * 8;
constexpr u32 WS_X3 = WS_X2 + 32 * 8 * 256 * 8;
constexpr u32 WS_END = WS_X3 + 2 * 128 * 320 * 8;
constexpr u32 WS_STAMP = 4u << 20;

constexpr u32 SPIN_CAP = 1u << 16;

#define ND1 3
#define G2_SLEEP 8

template <int CTRL> __device__ __forceinline__ float dpp(float x) {
  return __builtin_bit_cast(float, __builtin_amdgcn_mov_dpp(__builtin_bit_cast(int, x), CTRL, 0xf, 0xf, true));
}
constexpr int XOR1 = 0xB1, XOR2 = 0x4E, HMIR = 0x141, ROR8 = 0x128, ROR4 = 0x124;
__device__ __forceinline__ float swap16_max(float x) {
  auto s = __builtin_amdgcn_permlane16_swap(__float_as_uint(x), __float_as_uint(x), false, false);
  return fmaxf(__uint_as_float(s[0]), __uint_as_float(s[1]));
}
__device__ __forceinline__ float swap32_max(float x) {
  auto s = __builtin_amdgcn_permlane32_swap(__float_as_uint(x), __float_as_uint(x), false, false);
  return fmaxf(__uint_as_float(s[0]), __uint_as_float(s[1]));
}
__device__ __forceinline__ float swap16_sum(float x) {
  auto s = __builtin_amdgcn_permlane16_swap(__float_as_uint(x), __float_as_uint(x), false, false);
  return __uint_as_float(s[0]) + __uint_as_float(s[1]);
}
__device__ __forceinline__ float swap32_sum(float x) {
  auto s = __builtin_amdgcn_permlane32_swap(__float_as_uint(x), __float_as_uint(x), false, false);
  return __uint_as_float(s[0]) + __uint_as_float(s[1]);
}
__device__ __forceinline__ u32 pk_bf16(float lo, float hi) {
  bf16x2 z = {(__bf16)lo, (__bf16)hi};
  return __builtin_bit_cast(u32, z);
}
__device__ __forceinline__ float dot2(u32 a, u32 b, float c) {
  return __builtin_amdgcn_fdot2_f32_bf16(__builtin_bit_cast(bf16x2, a), __builtin_bit_cast(bf16x2, b), c, false);
}
__device__ __forceinline__ f32x4 sel_mfma(u32 cdup, const v4u& msk, const v4u& tile, f32x4 acc) {
  v4u a = {cdup & msk.x, cdup & msk.y, cdup & msk.z, cdup & msk.w};
  return __builtin_amdgcn_mfma_f32_16x16x32_bf16(__builtin_bit_cast(bf16x8, a), __builtin_bit_cast(bf16x8, tile), acc, 0, 0, 0);
}
__device__ __forceinline__ void st_granule(const char* base, u32 off, float v, u32 tag) {
  __hip_atomic_store((GAS u64*)(base + off), ((u64)tag << 32) | __float_as_uint(v), __ATOMIC_RELAXED, __HIP_MEMORY_SCOPE_AGENT);
}
#define BAR() asm volatile("s_waitcnt lgkmcnt(0)\n\ts_barrier" ::: "memory")
#define LDT(dst, voff, base, imm) asm volatile("global_load_dwordx4 %0, %1, %2 offset:" #imm " nt" : "=v"(dst) : "v"(voff), "s"(base))

#define STAMP(n) do { __builtin_amdgcn_sched_barrier(0); stamp[n] = __builtin_amdgcn_s_memrealtime(); __builtin_amdgcn_sched_barrier(0); } while (0)

struct Lds {
  float pa[8 * 2 * 64];
  float po[8 * 2 * 192];
  float ml[16];
  float zp[8];
  u32 cd[64];
  u32 qs[96];
};

__global__ void __launch_bounds__(512, 2)
fused_decode_kernel(const char* __restrict__ x, const char* __restrict__ kc, const char* __restrict__ vc,
                    const char* __restrict__ wq, const char* __restrict__ wo, const char* __restrict__ wg,
                    const char* __restrict__ wu, const char* __restrict__ wd, const char* __restrict__ gw,
                    char* __restrict__ out, char* __restrict__ ws) {
  __shared__ __attribute__((aligned(16))) Lds lds;
  u64 stamp[16];
#pragma unroll
  for (int n = 0; n < 16; n++) stamp[n] = 0;
  STAMP(0);
  const u32 tid = threadIdx.x, lane = tid & 63;
  const u32 w = __builtin_amdgcn_readfirstlane(tid >> 6);
  const u32 b = blockIdx.x, Q = b & 7, i = b >> 3, h = 4 * Q + (i & 3), s = i >> 2, q4 = s >> 1, half = s & 1;
  const u32 kr = lane >> 3, ch = lane & 7;

  const u32 lc4 = w & 3, hf = w >> 2, isM = (lc4 == 3);
  const u32 im = i < 30 ? i : 29, lc6 = im % 6, rs = im / 6, mat = lc6 / 3, lcx = lc6 % 3;
  const u32 xrow0 = isM ? 128 * rs + 64 * hf : 80 * s + 40 * hf;
  const u32 RS = isM ? DF * 2 : HD * 2;
  const char* abase = isM ? (mat ? wu : wg) + (size_t)xrow0 * (DF * 2) + (3 * Q + lcx) * 128
                          : wq + (size_t)xrow0 * (HD * 2) + (3 * h + lc4) * 128;
  u32 xs, ep, xz, gz;
  {
    const u32 o1 = min(xrow0 / 2 + lane, 319u) * 4, o2 = (40 * w + min(lane, 39u)) * 4, z0 = 0;
    asm volatile("global_load_dword %0, %1, %2" : "=v"(xs) : "v"(o1), "s"(x));
    asm volatile("global_load_dword %0, %1, %2 sc1" : "=v"(ep) : "v"(z0), "s"(ws));
    asm volatile("global_load_dword %0, %1, %2" : "=v"(xz) : "v"(o2), "s"(x));
    asm volatile("global_load_dword %0, %1, %2" : "=v"(gz) : "v"(o2), "s"(gw));
  }
  v4u ta[8];
  {
    const u32 va = kr * RS + ch * 16, ts = 8 * RS;
    LDT(ta[0], va, abase, 0);
    LDT(ta[1], va + ts, abase, 0);
    LDT(ta[2], va + 2 * ts, abase, 0);
    LDT(ta[3], va + 3 * ts, abase, 0);
    LDT(ta[4], va + 4 * ts, abase, 0);
    asm volatile("s_cmp_lg_u32 %7, 3\n\ts_cbranch_scc1 1f\n\t"
                 "global_load_dwordx4 %0, %3, %6 nt\n\tglobal_load_dwordx4 %1, %4, %6 nt\n\tglobal_load_dwordx4 %2, %5, %6 nt\n1:"
                 : "=&v"(ta[5]), "=&v"(ta[6]), "=&v"(ta[7])
                 : "v"(va + 5 * ts), "v"(va + 6 * ts), "v"(va + 7 * ts), "s"(abase), "s"(lc4) : "scc");
  }
  v4u tk[6], tv[6];
  const u32 vkv = kr * 384 + ch * 16;
  const size_t kvoff = ((size_t)h * SK + (SK - WIN) + 128 * s + 16 * w) * 384;
  const char* kbase = kc + kvoff;
  const char* vbase = vc + kvoff;
  LDT(tk[0], vkv, kbase, 0);
  LDT(tk[1], vkv, kbase, 128);
  LDT(tk[2], vkv, kbase, 256);
  LDT(tk[3], vkv, kbase, 3072);
  STAMP(1);

  const u32 hm = (ch & 1) ? 0xffff0000u : 0x0000ffffu;
  const v4u msk = {(ch >> 1) == 0 ? hm : 0u, (ch >> 1) == 1 ? hm : 0u, (ch >> 1) == 2 ? hm : 0u, (ch >> 1) == 3 ? hm : 0u};

  asm volatile("s_waitcnt vmcnt(12)\n\ts_cmp_eq_u32 %4, 3\n\ts_cbranch_scc1 1f\n\ts_waitcnt vmcnt(9)\n1:"
               : "+v"(xs), "+v"(ep), "+v"(xz), "+v"(gz) : "s"(lc4) : "scc");
  const u32 tag = __builtin_amdgcn_readfirstlane(ep) + 1u;
  u32 xd[8];
  {
    const u32 psel = (kr & 1) ? 0x03020302u : 0x01000100u;
#pragma unroll
    for (int G = 0; G < 8; G++) {
      const u32 d = __builtin_amdgcn_ds_bpermute((4 * G + (kr >> 1)) * 4, xs);
      xd[G] = __builtin_amdgcn_perm(d, d, psel);
    }
  }
  {
    float z = lane < 40 ? dot2(xz, gz, 0.f) : 0.f;
    z += dpp<XOR1>(z); z += dpp<XOR2>(z); z += dpp<HMIR>(z); z += dpp<ROR8>(z);
    z = swap32_sum(swap16_sum(z));
    if (lane == 0) lds.zp[w] = z;
  }
  STAMP(2);
  asm volatile("s_waitcnt vmcnt(4)"
               : "+v"(ta[0]), "+v"(ta[1]), "+v"(ta[2]), "+v"(ta[3]), "+v"(ta[4]), "+v"(ta[5]), "+v"(ta[6]), "+v"(ta[7]));
  STAMP(3);
  {
    f32x4 acc = {0.f, 0.f, 0.f, 0.f};
#pragma unroll
    for (int G = 0; G < 5; G++) acc = sel_mfma(xd[G], msk, ta[G], acc);
    if (isM) {
#pragma unroll
      for (int G = 5; G < 8; G++) acc = sel_mfma(xd[G], msk, ta[G], acc);
    }
    if ((lane >> 5) == ((lane >> 3) & 1))
      *(f32x4*)&lds.pa[(w * 2 + (lane >> 5)) * 64 + 8 * ch + 4 * ((lane >> 4) & 1)] = acc;
  }
  BAR();
  if (w < 4) {
    const float v = (lds.pa[(w * 2) * 64 + lane] + lds.pa[(w * 2 + 1) * 64 + lane]) +
                    (lds.pa[(w * 2 + 8) * 64 + lane] + lds.pa[(w * 2 + 9) * 64 + lane]);
    if (w < 3) st_granule(ws + WS_X1Q + (h * 8 + s) * 1536 + w * 512, lane * 8, v, tag);
    else if (i < 30) st_granule(ws + WS_X1M + (Q * 10 + mat * 5 + rs) * 1536 + lcx * 512, lane * 8, v, tag);
  }
  STAMP(4);
  LDT(tk[4], vkv, kbase, 3200);
  LDT(tk[5], vkv, kbase, 3328);
  LDT(tv[0], vkv, vbase, 0);
  LDT(tv[1], vkv, vbase, 128);
  LDT(tv[2], vkv, vbase, 256);
  const u32 kq = 4 * (h & 3) + q4;
  const char* g1base = (w < 3 ? ws + WS_X1Q + h * (8 * 1536) + w * 512 : ws + WS_X1M + Q * (10 * 1536)) + 3072;
  const u32 g1off = w < 3 ? lane * 8 : (12 * kq + min(lane, 11u)) * 8;
  const u32 g1off2 = g1off + 7680;
  v2u g1[10];
#define G1_LOADS()                                                                                                          \
  asm volatile("s_cmp_gt_u32 %13, 3\n\ts_cbranch_scc1 1f\n\t"                                                               \
               "global_load_dwordx2 %0, %10, %12 offset:-3072 sc1\n\tglobal_load_dwordx2 %1, %10, %12 offset:-1536 sc1\n\t" \
               "global_load_dwordx2 %2, %10, %12 sc1\n\tglobal_load_dwordx2 %3, %10, %12 offset:1536 sc1\n\t"               \
               "global_load_dwordx2 %4, %10, %12 offset:3072 sc1\n\tglobal_load_dwordx2 %5, %11, %12 offset:-3072 sc1\n\t"  \
               "global_load_dwordx2 %6, %11, %12 offset:-1536 sc1\n\tglobal_load_dwordx2 %7, %11, %12 sc1\n\t"              \
               "s_cmp_lg_u32 %13, 3\n\ts_cbranch_scc1 1f\n\t"                                                               \
               "global_load_dwordx2 %8, %11, %12 offset:1536 sc1\n\tglobal_load_dwordx2 %9, %11, %12 offset:3072 sc1\n1:"   \
               : "=&v"(g1[0]), "=&v"(g1[1]), "=&v"(g1[2]), "=&v"(g1[3]), "=&v"(g1[4]), "=&v"(g1[5]), "=&v"(g1[6]),          \
                 "=&v"(g1[7]), "=&v"(g1[8]), "=&v"(g1[9])                                                                   \
               : "v"(g1off), "v"(g1off2), "s"(g1base), "s"(w) : "scc")
  G1_LOADS();
  LDT(tv[3], vkv, vbase, 3072);
  LDT(tv[4], vkv, vbase, 3200);
  LDT(tv[5], vkv, vbase, 3328);
  const u32 isD = w >= 3;
  const u32 line = 5 * half + (isD ? w - 3 : 0);
  const char* obase = wo + (size_t)(DH * h + 48 * q4) * 1280 + line * 128;
  const char* dbase = wd + (size_t)(DH * Q + 12 * kq) * 1280 + line * 128;
  const u32 vd = kr * 1280 + ch * 16;
  v4u td[8];
#define LDT_IF_D(dst, voff, base)                                                                \
  asm volatile("s_cmp_lt_u32 %3, 3\n\ts_cbranch_scc1 1f\n\tglobal_load_dwordx4 %0, %1, %2 nt\n1:" \
               : "=v"(dst) : "v"(voff), "s"(base), "s"(w) : "scc")
#pragma unroll
  for (int G = 0; G < ND1; G++) LDT_IF_D(td[G], vd + G * 10240, obase);
  STAMP(5);

  asm volatile("s_waitcnt vmcnt(%c19)\n\ts_cmp_ge_u32 %18, 3\n\ts_cbranch_scc1 1f\n\ts_waitcnt vmcnt(3)\n1:"
               : "+v"(tk[0]), "+v"(tk[1]), "+v"(tk[2]), "+v"(tk[3]), "+v"(tk[4]), "+v"(tk[5]), "+v"(tv[0]), "+v"(tv[1]), "+v"(tv[2]),
                 "+v"(g1[0]), "+v"(g1[1]), "+v"(g1[2]), "+v"(g1[3]), "+v"(g1[4]), "+v"(g1[5]), "+v"(g1[6]), "+v"(g1[7]), "+v"(g1[8])
               : "s"(w), "i"(3 + ND1) : "scc");
  asm volatile("" : "+v"(g1[9]));
  STAMP(6);
  if (w < 4) {
    const int n = w < 3 ? 8 : 10;
    for (u32 spin = 0; spin < SPIN_CAP; spin++) {
      bool ok = g1[0].y == tag && g1[1].y == tag && g1[2].y == tag && g1[3].y == tag && g1[4].y == tag && g1[5].y == tag &&
                g1[6].y == tag && g1[7].y == tag;
      if (n == 10) ok = ok && g1[8].y == tag && g1[9].y == tag;
      if (__all(ok)) break;
      G1_LOADS();
      asm volatile("s_waitcnt vmcnt(0)"
                   : "+v"(g1[0]), "+v"(g1[1]), "+v"(g1[2]), "+v"(g1[3]), "+v"(g1[4]), "+v"(g1[5]), "+v"(g1[6]), "+v"(g1[7]),
                     "+v"(g1[8]), "+v"(g1[9]));
    }
#define F(j) __uint_as_float(g1[j].x)
    if (w < 3) {
      const float qv = ((F(0) + F(1)) + (F(2) + F(3))) + ((F(4) + F(5)) + (F(6) + F(7)));
      ((unsigned short*)lds.qs)[w * 64 + lane] = (unsigned short)pk_bf16(qv, qv);
    } else {
      const float g = ((F(0) + F(1)) + (F(2) + F(3))) + F(4), u = ((F(5) + F(6)) + (F(7) + F(8))) + F(9);
      const float z = ((lds.zp[0] + lds.zp[1]) + (lds.zp[2] + lds.zp[3])) + ((lds.zp[4] + lds.zp[5]) + (lds.zp[6] + lds.zp[7]));
      const float omg = 1.f - __builtin_amdgcn_rcpf(1.f + __builtin_amdgcn_exp2f(-1.4426950408889634f * z));
      const float y2 = 2.f * 0.7978845608028654f * 1.4426950408889634f * (g + 0.044715f * g * g * g);
      const float ge = g - g * __builtin_amdgcn_rcpf(1.f + __builtin_amdgcn_exp2f(y2));
      const float hv = lane < 12 ? ge * u * omg : 0.f;
      if (lane < 16) lds.cd[(lane & 7) * 8 + 6 + (lane >> 3)] = pk_bf16(hv, hv);
    }
#undef F
  }
  BAR();
  STAMP(7);
  f32x4 ao[3];
  float mw, lw;
  {
    const v4u q0 = *(const v4u*)&lds.qs[4 * ch], q1 = *(const v4u*)&lds.qs[32 + 4 * ch], q2 = *(const v4u*)&lds.qs[64 + 4 * ch];
    float s0 = 0.f, s1 = 0.f;
#pragma unroll
    for (int e = 0; e < 4; e++) { s0 = dot2(tk[0][e], q0[e], s0); s1 = dot2(tk[3][e], q0[e], s1); }
#pragma unroll
    for (int e = 0; e < 4; e++) { s0 = dot2(tk[1][e], q1[e], s0); s1 = dot2(tk[4][e], q1[e], s1); }
#pragma unroll
    for (int e = 0; e < 4; e++) { s0 = dot2(tk[2][e], q2[e], s0); s1 = dot2(tk[5][e], q2[e], s1); }
    s0 += dpp<XOR1>(s0); s1 += dpp<XOR1>(s1);
    s0 += dpp<XOR2>(s0); s1 += dpp<XOR2>(s1);
    s0 += dpp<HMIR>(s0); s1 += dpp<HMIR>(s1);
    constexpr float C2 = 0.07216878364870322f * 1.4426950408889634f;
    s0 *= C2; s1 *= C2;
    float m = fmaxf(s0, s1);
    m = fmaxf(m, dpp<ROR8>(m));
    m = swap32_max(swap16_max(m));
    const float p0 = __builtin_amdgcn_exp2f(s0 - m), p1 = __builtin_amdgcn_exp2f(s1 - m);
    const u32 pd0 = pk_bf16(p0, p0), pd1 = pk_bf16(p1, p1);
    float l = __uint_as_float(pd0 & 0xffff0000u) + __uint_as_float(pd1 & 0xffff0000u);
    l += dpp<ROR8>(l);
    l = swap32_sum(swap16_sum(l));
    mw = m; lw = l;
    const f32x4 zero = {0.f, 0.f, 0.f, 0.f};
#pragma unroll
    for (int e = 0; e < 3; e++) ao[e] = sel_mfma(pd0, msk, tv[e], zero);
    STAMP(8);
    asm volatile("s_waitcnt vmcnt(%c4)\n\ts_cmp_ge_u32 %3, 3\n\ts_cbranch_scc1 1f\n\ts_waitcnt vmcnt(0)\n1:"
                 : "+v"(tv[3]), "+v"(tv[4]), "+v"(tv[5]) : "s"(w), "i"(ND1) : "scc");
#pragma unroll
    for (int G = ND1; G < 6; G++) LDT_IF_D(td[G], vd + G * 10240, obase);
    LDT_IF_D(td[6], vd, dbase);
    LDT_IF_D(td[7], (kr & 3) * 1280 + ch * 16 + 10240, dbase);
#pragma unroll
    for (int e = 0; e < 3; e++) ao[e] = sel_mfma(pd1, msk, tv[3 + e], ao[e]);
  }
  if ((lane >> 5) == ((lane >> 3) & 1)) {
    float* p = &lds.po[(w * 2 + (lane >> 5)) * 192 + 8 * ch + 4 * ((lane >> 4) & 1)];
    *(f32x4*)p = ao[0];
    *(f32x4*)(p + 64) = ao[1];
    *(f32x4*)(p + 128) = ao[2];
  }
  if (lane == 0) { lds.ml[w] = mw; lds.ml[8 + w] = lw; }
  BAR();
  STAMP(9);
  if (w < 4) {
    const f32x4 m0 = *(const f32x4*)&lds.ml[0], m1 = *(const f32x4*)&lds.ml[4];
    const f32x4 l0 = *(const f32x4*)&lds.ml[8], l1 = *(const f32x4*)&lds.ml[12];
    const float M = fmaxf(fmaxf(fmaxf(m0.x, m0.y), fmaxf(m0.z, m0.w)), fmaxf(fmaxf(m1.x, m1.y), fmaxf(m1.z, m1.w)));
    float f[8];
#pragma unroll
    for (int e = 0; e < 4; e++) { f[e] = __builtin_amdgcn_exp2f(m0[e] - M); f[4 + e] = __builtin_amdgcn_exp2f(m1[e] - M); }
    float v;
    if (w < 3) {
      const float* p = &lds.po[w * 64 + lane];
      v = 0.f;
#pragma unroll
      for (int e = 0; e < 8; e++) v = fmaf(f[e], p[(2 * e) * 192] + p[(2 * e + 1) * 192], v);
    } else {
      float L = 0.f;
#pragma unroll
      for (int e = 0; e < 4; e++) L = fmaf(f[e], l0[e], fmaf(f[4 + e], l1[e], L));
      v = lane == 0 ? M : L;
    }
    if (w < 3 || lane < 2) st_granule(ws + WS_X2 + (h * 8 + s) * 2048 + w * 512, lane * 8, v, tag);
  }
  STAMP(10);
  if (w == 0) {
    const char* g2base = ws + WS_X2 + h * (8 * 2048);
    const u32 go = (lane < 48 ? 48 * q4 + lane : 192 + (lane & 1)) * 8;
    const u32 gm = (lane & 7) * 2048 + (192 + ((lane >> 3) & 1)) * 8;
    v2u g2[9];
    __builtin_amdgcn_s_sleep(G2_SLEEP);
    for (u32 spin = 0; spin < SPIN_CAP; spin++) {
      asm volatile("global_load_dwordx2 %0, %9, %12 sc1\n\tglobal_load_dwordx2 %1, %9, %12 offset:2048 sc1\n\t"
                   "global_load_dwordx2 %2, %10, %12 offset:-4096 sc1\n\tglobal_load_dwordx2 %3, %10, %12 offset:-2048 sc1\n\t"
                   "global_load_dwordx2 %4, %10, %12 sc1\n\tglobal_load_dwordx2 %5, %10, %12 offset:2048 sc1\n\t"
                   "global_load_dwordx2 %6, %11, %12 offset:-2048 sc1\n\tglobal_load_dwordx2 %7, %11, %12 sc1\n\t"
                   "global_load_dwordx2 %8, %13, %12 sc1\n\ts_waitcnt vmcnt(0)"
                   : "=&v"(g2[0]), "=&v"(g2[1]), "=&v"(g2[2]), "=&v"(g2[3]), "=&v"(g2[4]), "=&v"(g2[5]), "=&v"(g2[6]),
                     "=&v"(g2[7]), "=&v"(g2[8])
                   : "v"(go), "v"(go + 4 * 2048), "v"(go + 7 * 2048), "s"(g2base), "v"(gm));
      const bool ok = g2[0].y == tag && g2[1].y == tag && g2[2].y == tag && g2[3].y == tag && g2[4].y == tag &&
                      g2[5].y == tag && g2[6].y == tag && g2[7].y == tag && g2[8].y == tag;
      if (__all(ok)) break;
    }
    STAMP(11);
    const float ms = __uint_as_float(g2[8].x);
    float M = ms;
    M = fmaxf(M, dpp<XOR1>(M)); M = fmaxf(M, dpp<XOR2>(M)); M = fmaxf(M, dpp<HMIR>(M));
    const float fs = __builtin_amdgcn_exp2f(ms - M);
    float Lp = fs * dpp<ROR8>(ms);
    Lp += dpp<XOR1>(Lp); Lp += dpp<XOR2>(Lp); Lp += dpp<HMIR>(Lp);
    const float z = ((lds.zp[0] + lds.zp[1]) + (lds.zp[2] + lds.zp[3])) + ((lds.zp[4] + lds.zp[5]) + (lds.zp[6] + lds.zp[7]));
    const float gate = __builtin_amdgcn_rcpf(1.f + __builtin_amdgcn_exp2f(-1.4426950408889634f * z));
    const float cs = fs * gate * __builtin_amdgcn_rcpf(Lp);
    float o = 0.f;
#pragma unroll
    for (int e = 0; e < 8; e++)
      o = fmaf(__builtin_bit_cast(float, __builtin_amdgcn_readlane(__builtin_bit_cast(int, cs), e)), __uint_as_float(g2[e].x), o);
    if (lane < 48) lds.cd[(lane & 7) * 8 + (lane >> 3)] = pk_bf16(o, o);
  }
  asm volatile("s_waitcnt vmcnt(0)"
               : "+v"(td[0]), "+v"(td[1]), "+v"(td[2]), "+v"(td[3]), "+v"(td[4]), "+v"(td[5]), "+v"(td[6]), "+v"(td[7]));
  BAR();
  STAMP(12);
  if (isD) {
    const v4u c0 = *(const v4u*)&lds.cd[kr * 8], c1 = *(const v4u*)&lds.cd[kr * 8 + 4];
    f32x4 acc = {0.f, 0.f, 0.f, 0.f};
#pragma unroll
    for (int G = 0; G < 4; G++) acc = sel_mfma(c0[G], msk, td[G], acc);
#pragma unroll
    for (int G = 0; G < 4; G++) acc = sel_mfma(c1[G], msk, td[4 + G], acc);
    float r[4];
#pragma unroll
    for (int e = 0; e < 4; e++) {
      const u32 u = __float_as_uint(dpp<ROR8>(acc[e]));
      auto sw = __builtin_amdgcn_permlane32_swap(u, u, false, false);
      r[e] = acc[e] + __uint_as_float(sw[1]);
    }
    if (lane < 32 && !(lane & 8)) {
      const char* x3 = ws + WS_X3 + ((half * 128 + h * 4 + q4) * 320 + (w - 3) * 64) * 8;
      const u32 off = (8 * ch + 4 * (lane >> 4)) * 8;
      const v4u a = {__float_as_uint(r[0]), tag, __float_as_uint(r[1]), tag};
      const v4u c = {__float_as_uint(r[2]), tag, __float_as_uint(r[3]), tag};
      asm volatile("global_store_dwordx4 %0, %1, %3 sc1\n\tglobal_store_dwordx4 %0, %2, %3 offset:16 sc1\n\ts_nop 1"
                   :: "v"(off), "v"(a), "v"(c), "s"(x3) : "memory");
    }
    STAMP(13);
  } else if (w == 0 && b < 80) {
    const u32 hr = b / 40, rr = b % 40;
    const char* x3 = ws + WS_X3 + (hr * 128 * 320 + 8 * rr) * 8;
    const u32 o0 = (lane >> 2) * 2560 + (lane & 3) * 16;
    v4u g3[8];
    for (u32 spin = 0; spin < SPIN_CAP; spin++) {
      asm volatile("global_load_dwordx4 %0, %8, %16 sc1\n\tglobal_load_dwordx4 %1, %9, %16 sc1\n\t"
                   "global_load_dwordx4 %2, %10, %16 sc1\n\tglobal_load_dwordx4 %3, %11, %16 sc1\n\t"
                   "global_load_dwordx4 %4, %12, %16 sc1\n\tglobal_load_dwordx4 %5, %13, %16 sc1\n\t"
                   "global_load_dwordx4 %6, %14, %16 sc1\n\tglobal_load_dwordx4 %7, %15, %16 sc1\n\ts_waitcnt vmcnt(0)"
                   : "=&v"(g3[0]), "=&v"(g3[1]), "=&v"(g3[2]), "=&v"(g3[3]), "=&v"(g3[4]), "=&v"(g3[5]), "=&v"(g3[6]), "=&v"(g3[7])
                   : "v"(o0), "v"(o0 + 40960), "v"(o0 + 2 * 40960), "v"(o0 + 3 * 40960), "v"(o0 + 4 * 40960),
                     "v"(o0 + 5 * 40960), "v"(o0 + 6 * 40960), "v"(o0 + 7 * 40960), "s"(x3));
      bool ok = true;
#pragma unroll
      for (int e = 0; e < 8; e++) ok = ok && g3[e].y == tag && g3[e].w == tag;
      if (__all(ok)) break;
    }
    STAMP(13);
    float a0 = 0.f, a1 = 0.f;
#pragma unroll
    for (int e = 0; e < 8; e++) { a0 += __uint_as_float(g3[e].x); a1 += __uint_as_float(g3[e].z); }
    a0 += dpp<ROR4>(a0); a1 += dpp<ROR4>(a1);
    a0 += dpp<ROR8>(a0); a1 += dpp<ROR8>(a1);
    a0 = swap32_sum(swap16_sum(a0)); a1 = swap32_sum(swap16_sum(a1));
    if (lane < 4) *(GAS u32*)(out + (8 * b + 2 * lane) * 2) = pk_bf16(a0, a1);
    if (b == 0 && lane == 0) __hip_atomic_store((GAS u32*)(ws + WS_EPOCH), tag, __ATOMIC_RELAXED, __HIP_MEMORY_SCOPE_AGENT);
  }
  STAMP(14);
  if (b == 0 && w == 0) {
    const u64 d = (stamp[1] - stamp[0]) * 20;
    const u64 t0 = __builtin_amdgcn_s_memrealtime();
    while (__builtin_amdgcn_s_memrealtime() - t0 < d) __builtin_amdgcn_s_sleep(1);
  }
}

}

extern "C" void kernel_launch(void* const* d_in, const int* in_sizes, int n_in, void* d_out, int out_size, void* d_ws,
                              size_t ws_size, hipStream_t stream) {
  fused_decode_kernel<<<256, 512, 0, stream>>>(
      (const char*)d_in[0], (const char*)d_in[1], (const char*)d_in[2], (const char*)d_in[3], (const char*)d_in[4],
      (const char*)d_in[6], (const char*)d_in[7], (const char*)d_in[8], (const char*)d_in[9], (char*)d_out, (char*)d_ws);
}
```

```cpp
#include <hip/hip_runtime.h>
#include <hip/hip_bf16.h>
#include <stdint.h>

namespace {

typedef unsigned u32;
typedef unsigned long long u64;
typedef unsigned v4u __attribute__((ext_vector_type(4)));
typedef unsigned v2u __attribute__((ext_vector_type(2)));
typedef float f32x4 __attribute__((ext_vector_type(4)));
typedef short bf16x8 __attribute__((ext_vector_type(8)));
typedef __bf16 bf16x2 __attribute__((ext_vector_type(2)));
#define GAS __attribute__((address_space(1)))

constexpr int DM = 640, NH = 32, SK = 4096, DH = 192, WIN = 1024, DF = 1536, HD = NH * DH;

constexpr u32 WS_EPOCH = 0;
constexpr u32 WS_X1Q = 4096;
constexpr u32 WS_X1M = WS_X1Q + 32 * 8 * 192 * 8;
constexpr u32 WS_X2 = WS_X1M + 8 * 10 * 192 * 8;
constexpr u32 WS_X3 = WS_X2 + 32 * 8 * 256 * 8;
constexpr u32 WS_END = WS_X3 + 2 * 128 * 320 * 8;
constexpr u32 WS_STAMP = 4u << 20;

constexpr u32 SPIN_CAP = 1u << 16;

#define ND1 3
#define G2_SLEEP 8

template <int CTRL> __device__ __forceinline__ float dpp(float x) {
  return __builtin_bit_cast(float, __builtin_amdgcn_mov_dpp(__builtin_bit_cast(int, x), CTRL, 0xf, 0xf, true));
}
constexpr int XOR1 = 0xB1, XOR2 = 0x4E, HMIR = 0x141, ROR8 = 0x128, ROR4 = 0x124;
__device__ __forceinline__ float swap16_max(float x) {
  auto s = __builtin_amdgcn_permlane16_swap(__float_as_uint(x), __float_as_uint(x), false, false);
  return fmaxf(__uint_as_float(s[0]), __uint_as_float(s[1]));
}
__device__ __forceinline__ float swap32_max(float x) {
  auto s = __builtin_amdgcn_permlane32_swap(__float_as_uint(x), __float_as_uint(x), false, false);
  return fmaxf(__uint_as_float(s[0]), __uint_as_float(s[1]));
}
__device__ __forceinline__ float swap16_sum(float x) {
  auto s = __builtin_amdgcn_permlane16_swap(__float_as_uint(x), __float_as_uint(x), false, false);
  return __uint_as_float(s[0]) + __uint_as_float(s[1]);
}
__device__ __forceinline__ float swap32_sum(float x) {
  auto s = __builtin_amdgcn_permlane32_swap(__float_as_uint(x), __float_as_uint(x), false, false);
  return __uint_as_float(s[0]) + __uint_as_float(s[1]);
}
__device__ __forceinline__ u32 pk_bf16(float lo, float hi) {
  bf16x2 z = {(__bf16)lo, (__bf16)hi};
  return __builtin_bit_cast(u32, z);
}
__device__ __forceinline__ float dot2(u32 a, u32 b, float c) {
  return __builtin_amdgcn_fdot2_f32_bf16(__builtin_bit_cast(bf16x2, a), __builtin_bit_cast(bf16x2, b), c, false);
}
__device__ __forceinline__ f32x4 sel_mfma(u32 cdup, const v4u& msk, const v4u& tile, f32x4 acc) {
  v4u a = {cdup & msk.x, cdup & msk.y, cdup & msk.z, cdup & msk.w};
  return __builtin_amdgcn_mfma_f32_16x16x32_bf16(__builtin_bit_cast(bf16x8, a), __builtin_bit_cast(bf16x8, tile), acc, 0, 0, 0);
}
__device__ __forceinline__ void st_granule(const char* base, u32 off, float v, u32 tag) {
  __hip_atomic_store((GAS u64*)(base + off), ((u64)tag << 32) | __float_as_uint(v), __ATOMIC_RELAXED, __HIP_MEMORY_SCOPE_AGENT);
}
#define BAR() asm volatile("s_waitcnt lgkmcnt(0)\n\ts_barrier" ::: "memory")
#define LDT(dst, voff, base, imm) asm volatile("global_load_dwordx4 %0, %1, %2 offset:" #imm " nt" : "=v"(dst) : "v"(voff), "s"(base))

#define STAMP(n) do { __builtin_amdgcn_sched_barrier(0); stamp[n] = __builtin_amdgcn_s_memrealtime(); __builtin_amdgcn_sched_barrier(0); } while (0)

struct Lds {
  float pa[8 * 2 * 64];
  float po[8 * 2 * 192];
  float ml[16];
  float zp[8];
  u32 cd[64];
  u32 qs[96];
};

__global__ void __launch_bounds__(512, 2)
fused_decode_kernel(const char* __restrict__ x, const char* __restrict__ kc, const char* __restrict__ vc,
                    const char* __restrict__ wq, const char* __restrict__ wo, const char* __restrict__ wg,
                    const char* __restrict__ wu, const char* __restrict__ wd, const char* __restrict__ gw,
                    char* __restrict__ out, char* __restrict__ ws) {
  __shared__ __attribute__((aligned(16))) Lds lds;
  u64 stamp[16];
#pragma unroll
  for (int n = 0; n < 16; n++) stamp[n] = 0;
  STAMP(0);
  const u32 tid = threadIdx.x, lane = tid & 63;
  const u32 w = __builtin_amdgcn_readfirstlane(tid >> 6);
  const u32 b = blockIdx.x, Q = b & 7, i = b >> 3, h = 4 * Q + (i & 3), s = i >> 2, q4 = s >> 1, half = s & 1;
  const u32 kr = lane >> 3, ch = lane & 7;

  const u32 lc4 = w & 3, hf = w >> 2, isM = (lc4 == 3);
  const u32 im = i < 30 ? i : 29, lc6 = im % 6, rs = im / 6, mat = lc6 / 3, lcx = lc6 % 3;
  const u32 xrow0 = isM ? 128 * rs + 64 * hf : 80 * s + 40 * hf;
  const u32 RS = isM ? DF * 2 : HD * 2;
  const char* abase = isM ? (mat ? wu : wg) + (size_t)xrow0 * (DF * 2) + (3 * Q + lcx) * 128
                          : wq + (size_t)xrow0 * (HD * 2) + (3 * h + lc4) * 128;
  u32 xs, ep, xz, gz;
  {
    const u32 o1 = min(xrow0 / 2 + lane, 319u) * 4, o2 = (40 * w + min(lane, 39u)) * 4, z0 = 0;
    asm volatile("global_load_dword %0, %1, %2" : "=v"(xs) : "v"(o1), "s"(x));
    asm volatile("global_load_dword %0, %1, %2 sc1" : "=v"(ep) : "v"(z0), "s"(ws));
    asm volatile("global_load_dword %0, %1, %2" : "=v"(xz) : "v"(o2), "s"(x));
    asm volatile("global_load_dword %0, %1, %2" : "=v"(gz) : "v"(o2), "s"(gw));
  }
  v4u ta[8];
  {
    const u32 va = kr * RS + ch * 16, ts = 8 * RS;
    LDT(ta[0], va, abase, 0);
    LDT(ta[1], va + ts, abase, 0);
    LDT(ta[2], va + 2 * ts, abase, 0);
    LDT(ta[3], va + 3 * ts, abase, 0);
    LDT(ta[4], va + 4 * ts, abase, 0);
    asm volatile("s_cmp_lg_u32 %7, 3\n\ts_cbranch_scc1 1f\n\t"
                 "global_load_dwordx4 %0, %3, %6 nt\n\tglobal_load_dwordx4 %1, %4, %6 nt\n\tglobal_load_dwordx4 %2, %5, %6 nt\n1:"
                 : "=&v"(ta[5]), "=&v"(ta[6]), "=&v"(ta[7])
                 : "v"(va + 5 * ts), "v"(va + 6 * ts), "v"(va + 7 * ts), "s"(abase), "s"(lc4) : "scc");
  }
  v4u tk[6], tv[6];
  const u32 vkv = kr * 384 + ch * 16;
  const size_t kvoff = ((size_t)h * SK + (SK - WIN) + 128 * s + 16 * w) * 384;
  const char* kbase = kc + kvoff;
  const char* vbase = vc + kvoff;
  LDT(tk[0], vkv, kbase, 0);
  LDT(tk[1], vkv, kbase, 128);
  LDT(tk[2], vkv, kbase, 256);
  LDT(tk[3], vkv, kbase, 3072);
  STAMP(1);

  const u32 hm = (ch & 1) ? 0xffff0000u : 0x0000ffffu;
  const v4u msk = {(ch >> 1) == 0 ? hm : 0u, (ch >> 1) == 1 ? hm : 0u, (ch >> 1) == 2 ? hm : 0u, (ch >> 1) == 3 ? hm : 0u};

  asm volatile("s_waitcnt vmcnt(12)\n\ts_cmp_eq_u32 %4, 3\n\ts_cbranch_scc1 1f\n\ts_waitcnt vmcnt(9)\n1:"
               : "+v"(xs), "+v"(ep), "+v"(xz), "+v"(gz) : "s"(lc4) : "scc");
  const u32 tag = __builtin_amdgcn_readfirstlane(ep) + 1u;
  if (w == 0 && lane == 0) __hip_atomic_store((GAS u64*)(ws + (3u << 20) + b * 8), ((u64)tag << 32) | (u32)stamp[0], __ATOMIC_RELAXED, __HIP_MEMORY_SCOPE_AGENT);
  u32 xd[8];
  {
    const u32 psel = (kr & 1) ? 0x03020302u : 0x01000100u;
#pragma unroll
    for (int G = 0; G < 8; G++) {
      const u32 d = __builtin_amdgcn_ds_bpermute((4 * G + (kr >> 1)) * 4, xs);
      xd[G] = __builtin_amdgcn_perm(d, d, psel);
    }
  }
  {
    float z = lane < 40 ? dot2(xz, gz, 0.f) : 0.f;
    z += dpp<XOR1>(z); z += dpp<XOR2>(z); z += dpp<HMIR>(z); z += dpp<ROR8>(z);
    z = swap32_sum(swap16_sum(z));
    if (lane == 0) lds.zp[w] = z;
  }
  STAMP(2);
  asm volatile("s_waitcnt vmcnt(4)"
               : "+v"(ta[0]), "+v"(ta[1]), "+v"(ta[2]), "+v"(ta[3]), "+v"(ta[4]), "+v"(ta[5]), "+v"(ta[6]), "+v"(ta[7]));
  STAMP(3);
  {
    f32x4 acc = {0.f, 0.f, 0.f, 0.f};
#pragma unroll
    for (int G = 0; G < 5; G++) acc = sel_mfma(xd[G], msk, ta[G], acc);
    if (isM) {
#pragma unroll
      for (int G = 5; G < 8; G++) acc = sel_mfma(xd[G], msk, ta[G], acc);
    }
    if ((lane >> 5) == ((lane >> 3) & 1))
      *(f32x4*)&lds.pa[(w * 2 + (lane >> 5)) * 64 + 8 * ch + 4 * ((lane >> 4) & 1)] = acc;
  }
  BAR();
  if (w < 4) {
    const float v = (lds.pa[(w * 2) * 64 + lane] + lds.pa[(w * 2 + 1) * 64 + lane]) +
                    (lds.pa[(w * 2 + 8) * 64 + lane] + lds.pa[(w * 2 + 9) * 64 + lane]);
    if (w < 3) st_granule(ws + WS_X1Q + (h * 8 + s) * 1536 + w * 512, lane * 8, v, tag);
    else if (i < 30) st_granule(ws + WS_X1M + (Q * 10 + mat * 5 + rs) * 1536 + lcx * 512, lane * 8, v, tag);
  }
  STAMP(4);
  LDT(tk[4], vkv, kbase, 3200);
  LDT(tk[5], vkv, kbase, 3328);
  LDT(tv[0], vkv, vbase, 0);
  LDT(tv[1], vkv, vbase, 128);
  LDT(tv[2], vkv, vbase, 256);
  const u32 kq = 4 * (h & 3) + q4;
  const char* g1base = (w < 3 ? ws + WS_X1Q + h * (8 * 1536) + w * 512 : ws + WS_X1M + Q * (10 * 1536)) + 3072;
  const u32 g1off = w < 3 ? lane * 8 : (12 * kq + min(lane, 11u)) * 8;
  const u32 g1off2 = g1off + 7680;
  v2u g1[10];
#define G1_LOADS()                                                                                                          \
  asm volatile("s_cmp_gt_u32 %13, 3\n\ts_cbranch_scc1 1f\n\t"                                                               \
               "global_load_dwordx2 %0, %10, %12 offset:-3072 sc1\n\tglobal_load_dwordx2 %1, %10, %12 offset:-1536 sc1\n\t" \
               "global_load_dwordx2 %2, %10, %12 sc1\n\tglobal_load_dwordx2 %3, %10, %12 offset:1536 sc1\n\t"               \
               "global_load_dwordx2 %4, %10, %12 offset:3072 sc1\n\tglobal_load_dwordx2 %5, %11, %12 offset:-3072 sc1\n\t"  \
               "global_load_dwordx2 %6, %11, %12 offset:-1536 sc1\n\tglobal_load_dwordx2 %7, %11, %12 sc1\n\t"              \
               "s_cmp_lg_u32 %13, 3\n\ts_cbranch_scc1 1f\n\t"                                                               \
               "global_load_dwordx2 %8, %11, %12 offset:1536 sc1\n\tglobal_load_dwordx2 %9, %11, %12 offset:3072 sc1\n1:"   \
               : "=&v"(g1[0]), "=&v"(g1[1]), "=&v"(g1[2]), "=&v"(g1[3]), "=&v"(g1[4]), "=&v"(g1[5]), "=&v"(g1[6]),          \
                 "=&v"(g1[7]), "=&v"(g1[8]), "=&v"(g1[9])                                                                   \
               : "v"(g1off), "v"(g1off2), "s"(g1base), "s"(w) : "scc")
  G1_LOADS();
  LDT(tv[3], vkv, vbase, 3072);
  LDT(tv[4], vkv, vbase, 3200);
  LDT(tv[5], vkv, vbase, 3328);
  const u32 isD = w >= 3;
  const u32 line = 5 * half + (isD ? w - 3 : 0);
  const char* obase = wo + (size_t)(DH * h + 48 * q4) * 1280 + line * 128;
  const char* dbase = wd + (size_t)(DH * Q + 12 * kq) * 1280 + line * 128;
  const u32 vd = kr * 1280 + ch * 16;
  v4u td[8];
#define LDT_IF_D(dst, voff, base)                                                                \
  asm volatile("s_cmp_lt_u32 %3, 3\n\ts_cbranch_scc1 1f\n\tglobal_load_dwordx4 %0, %1, %2 nt\n1:" \
               : "=v"(dst) : "v"(voff), "s"(base), "s"(w) : "scc")
#pragma unroll
  for (int G = 0; G < ND1; G++) LDT_IF_D(td[G], vd + G * 10240, obase);
  STAMP(5);

  asm volatile("s_waitcnt vmcnt(%c19)\n\ts_cmp_ge_u32 %18, 3\n\ts_cbranch_scc1 1f\n\ts_waitcnt vmcnt(3)\n1:"
               : "+v"(tk[0]), "+v"(tk[1]), "+v"(tk[2]), "+v"(tk[3]), "+v"(tk[4]), "+v"(tk[5]), "+v"(tv[0]), "+v"(tv[1]), "+v"(tv[2]),
                 "+v"(g1[0]), "+v"(g1[1]), "+v"(g1[2]), "+v"(g1[3]), "+v"(g1[4]), "+v"(g1[5]), "+v"(g1[6]), "+v"(g1[7]), "+v"(g1[8])
               : "s"(w), "i"(3 + ND1) : "scc");
  asm volatile("" : "+v"(g1[9]));
  STAMP(6);
  if (w < 4) {
    const int n = w < 3 ? 8 : 10;
    for (u32 spin = 0; spin < SPIN_CAP; spin++) {
      bool ok = g1[0].y == tag && g1[1].y == tag && g1[2].y == tag && g1[3].y == tag && g1[4].y == tag && g1[5].y == tag &&
                g1[6].y == tag && g1[7].y == tag;
      if (n == 10) ok = ok && g1[8].y == tag && g1[9].y == tag;
      if (__all(ok)) break;
      G1_LOADS();
      asm volatile("s_waitcnt vmcnt(0)"
                   : "+v"(g1[0]), "+v"(g1[1]), "+v"(g1[2]), "+v"(g1[3]), "+v"(g1[4]), "+v"(g1[5]), "+v"(g1[6]), "+v"(g1[7]),
                     "+v"(g1[8]), "+v"(g1[9]));
    }
#define F(j) __uint_as_float(g1[j].x)
    if (w < 3) {
      const float qv = ((F(0) + F(1)) + (F(2) + F(3))) + ((F(4) + F(5)) + (F(6) + F(7)));
      ((unsigned short*)lds.qs)[w * 64 + lane] = (unsigned short)pk_bf16(qv, qv);
    } else {
      const float g = ((F(0) + F(1)) + (F(2) + F(3))) + F(4), u = ((F(5) + F(6)) + (F(7) + F(8))) + F(9);
      const float z = ((lds.zp[0] + lds.zp[1]) + (lds.zp[2] + lds.zp[3])) + ((lds.zp[4] + lds.zp[5]) + (lds.zp[6] + lds.zp[7]));
      const float omg = 1.f - __builtin_amdgcn_rcpf(1.f + __builtin_amdgcn_exp2f(-1.4426950408889634f * z));
      const float y2 = 2.f * 0.7978845608028654f * 1.4426950408889634f * (g + 0.044715f * g * g * g);
      const float ge = g - g * __builtin_amdgcn_rcpf(1.f + __builtin_amdgcn_exp2f(y2));
      const float hv = lane < 12 ? ge * u * omg : 0.f;
      if (lane < 16) lds.cd[(lane & 7) * 8 + 6 + (lane >> 3)] = pk_bf16(hv, hv);
    }
#undef F
  }
  BAR();
  STAMP(7);
  f32x4 ao[3];
  float mw, lw;
  {
    const v4u q0 = *(const v4u*)&lds.qs[4 * ch], q1 = *(const v4u*)&lds.qs[32 + 4 * ch], q2 = *(const v4u*)&lds.qs[64 + 4 * ch];
    float s0 = 0.f, s1 = 0.f;
#pragma unroll
    for (int e = 0; e < 4; e++) { s0 = dot2(tk[0][e], q0[e], s0); s1 = dot2(tk[3][e], q0[e], s1); }
#pragma unroll
    for (int e = 0; e < 4; e++) { s0 = dot2(tk[1][e], q1[e], s0); s1 = dot2(tk[4][e], q1[e], s1); }
#pragma unroll
    for (int e = 0; e < 4; e++) { s0 = dot2(tk[2][e], q2[e], s0); s1 = dot2(tk[5][e], q2[e], s1); }
    s0 += dpp<XOR1>(s0); s1 += dpp<XOR1>(s1);
    s0 += dpp<XOR2>(s0); s1 += dpp<XOR2>(s1);
    s0 += dpp<HMIR>(s0); s1 += dpp<HMIR>(s1);
    constexpr float C2 = 0.07216878364870322f * 1.4426950408889634f;
    s0 *= C2; s1 *= C2;
    float m = fmaxf(s0, s1);
    m = fmaxf(m, dpp<ROR8>(m));
    m = swap32_max(swap16_max(m));
    const float p0 = __builtin_amdgcn_exp2f(s0 - m), p1 = __builtin_amdgcn_exp2f(s1 - m);
    const u32 pd0 = pk_bf16(p0, p0), pd1 = pk_bf16(p1, p1);
    float l = __uint_as_float(pd0 & 0xffff0000u) + __uint_as_float(pd1 & 0xffff0000u);
    l += dpp<ROR8>(l);
    l = swap32_sum(swap16_sum(l));
    mw = m; lw = l;
    const f32x4 zero = {0.f, 0.f, 0.f, 0.f};
#pragma unroll
    for (int e = 0; e < 3; e++) ao[e] = sel_mfma(pd0, msk, tv[e], zero);
    STAMP(8);
    asm volatile("s_waitcnt vmcnt(%c4)\n\ts_cmp_ge_u32 %3, 3\n\ts_cbranch_scc1 1f\n\ts_waitcnt vmcnt(0)\n1:"
                 : "+v"(tv[3]), "+v"(tv[4]), "+v"(tv[5]) : "s"(w), "i"(ND1) : "scc");
#pragma unroll
    for (int G = ND1; G < 6; G++) LDT_IF_D(td[G], vd + G * 10240, obase);
    LDT_IF_D(td[6], vd, dbase);
    LDT_IF_D(td[7], (kr & 3) * 1280 + ch * 16 + 10240, dbase);
#pragma unroll
    for (int e = 0; e < 3; e++) ao[e] = sel_mfma(pd1, msk, tv[3 + e], ao[e]);
  }
  if ((lane >> 5) == ((lane >> 3) & 1)) {
    float* p = &lds.po[(w * 2 + (lane >> 5)) * 192 + 8 * ch + 4 * ((lane >> 4) & 1)];
    *(f32x4*)p = ao[0];
    *(f32x4*)(p + 64) = ao[1];
    *(f32x4*)(p + 128) = ao[2];
  }
  if (lane == 0) { lds.ml[w] = mw; lds.ml[8 + w] = lw; }
  BAR();
  STAMP(9);
  if (w < 4) {
    const f32x4 m0 = *(const f32x4*)&lds.ml[0], m1 = *(const f32x4*)&lds.ml[4];
    const f32x4 l0 = *(const f32x4*)&lds.ml[8], l1 = *(const f32x4*)&lds.ml[12];
    const float M = fmaxf(fmaxf(fmaxf(m0.x, m0.y), fmaxf(m0.z, m0.w)), fmaxf(fmaxf(m1.x, m1.y), fmaxf(m1.z, m1.w)));
    float f[8];
#pragma unroll
    for (int e = 0; e < 4; e++) { f[e] = __builtin_amdgcn_exp2f(m0[e] - M); f[4 + e] = __builtin_amdgcn_exp2f(m1[e] - M); }
    float v;
    if (w < 3) {
      const float* p = &lds.po[w * 64 + lane];
      v = 0.f;
#pragma unroll
      for (int e = 0; e < 8; e++) v = fmaf(f[e], p[(2 * e) * 192] + p[(2 * e + 1) * 192], v);
    } else {
      float L = 0.f;
#pragma unroll
      for (int e = 0; e < 4; e++) L = fmaf(f[e], l0[e], fmaf(f[4 + e], l1[e], L));
      v = lane == 0 ? M : L;
    }
    if (w < 3 || lane < 2) st_granule(ws + WS_X2 + (h * 8 + s) * 2048 + w * 512, lane * 8, v, tag);
  }
  STAMP(10);
  if (w == 0) {
    const char* g2base = ws + WS_X2 + h * (8 * 2048);
    const u32 go = (lane < 48 ? 48 * q4 + lane : 192 + (lane & 1)) * 8;
    const u32 gm = (lane & 7) * 2048 + (192 + ((lane >> 3) & 1)) * 8;
    v2u g2[9];
    __builtin_amdgcn_s_sleep(G2_SLEEP);
    for (u32 spin = 0; spin < SPIN_CAP; spin++) {
      asm volatile("global_load_dwordx2 %0, %9, %12 sc1\n\tglobal_load_dwordx2 %1, %9, %12 offset:2048 sc1\n\t"
                   "global_load_dwordx2 %2, %10, %12 offset:-4096 sc1\n\tglobal_load_dwordx2 %3, %10, %12 offset:-2048 sc1\n\t"
                   "global_load_dwordx2 %4, %10, %12 sc1\n\tglobal_load_dwordx2 %5, %10, %12 offset:2048 sc1\n\t"
                   "global_load_dwordx2 %6, %11, %12 offset:-2048 sc1\n\tglobal_load_dwordx2 %7, %11, %12 sc1\n\t"
                   "global_load_dwordx2 %8, %13, %12 sc1\n\ts_waitcnt vmcnt(0)"
                   : "=&v"(g2[0]), "=&v"(g2[1]), "=&v"(g2[2]), "=&v"(g2[3]), "=&v"(g2[4]), "=&v"(g2[5]), "=&v"(g2[6]),
                     "=&v"(g2[7]), "=&v"(g2[8])
                   : "v"(go), "v"(go + 4 * 2048), "v"(go + 7 * 2048), "s"(g2base), "v"(gm));
      const bool ok = g2[0].y == tag && g2[1].y == tag && g2[2].y == tag && g2[3].y == tag && g2[4].y == tag &&
                      g2[5].y == tag && g2[6].y == tag && g2[7].y == tag && g2[8].y == tag;
      if (__all(ok)) break;
    }
    STAMP(11);
    const float ms = __uint_as_float(g2[8].x);
    float M = ms;
    M = fmaxf(M, dpp<XOR1>(M)); M = fmaxf(M, dpp<XOR2>(M)); M = fmaxf(M, dpp<HMIR>(M));
    const float fs = __builtin_amdgcn_exp2f(ms - M);
    float Lp = fs * dpp<ROR8>(ms);
    Lp += dpp<XOR1>(Lp); Lp += dpp<XOR2>(Lp); Lp += dpp<HMIR>(Lp);
    const float z = ((lds.zp[0] + lds.zp[1]) + (lds.zp[2] + lds.zp[3])) + ((lds.zp[4] + lds.zp[5]) + (lds.zp[6] + lds.zp[7]));
    const float gate = __builtin_amdgcn_rcpf(1.f + __builtin_amdgcn_exp2f(-1.4426950408889634f * z));
    const float cs = fs * gate * __builtin_amdgcn_rcpf(Lp);
    float o = 0.f;
#pragma unroll
    for (int e = 0; e < 8; e++)
      o = fmaf(__builtin_bit_cast(float, __builtin_amdgcn_readlane(__builtin_bit_cast(int, cs), e)), __uint_as_float(g2[e].x), o);
    if (lane < 48) lds.cd[(lane & 7) * 8 + (lane >> 3)] = pk_bf16(o, o);
  }
  asm volatile("s_waitcnt vmcnt(0)"
               : "+v"(td[0]), "+v"(td[1]), "+v"(td[2]), "+v"(td[3]), "+v"(td[4]), "+v"(td[5]), "+v"(td[6]), "+v"(td[7]));
  BAR();
  STAMP(12);
  if (isD) {
    const v4u c0 = *(const v4u*)&lds.cd[kr * 8], c1 = *(const v4u*)&lds.cd[kr * 8 + 4];
    f32x4 acc = {0.f, 0.f, 0.f, 0.f};
#pragma unroll
    for (int G = 0; G < 4; G++) acc = sel_mfma(c0[G], msk, td[G], acc);
#pragma unroll
    for (int G = 0; G < 4; G++) acc = sel_mfma(c1[G], msk, td[4 + G], acc);
    float r[4];
#pragma unroll
    for (int e = 0; e < 4; e++) {
      const u32 u = __float_as_uint(dpp<ROR8>(acc[e]));
      auto sw = __builtin_amdgcn_permlane32_swap(u, u, false, false);
      r[e] = acc[e] + __uint_as_float(sw[1]);
    }
    if (lane < 32 && !(lane & 8)) {
      const char* x3 = ws + WS_X3 + ((half * 128 + h * 4 + q4) * 320 + (w - 3) * 64) * 8;
      const u32 off = (8 * ch + 4 * (lane >> 4)) * 8;
      const v4u a = {__float_as_uint(r[0]), tag, __float_as_uint(r[1]), tag};
      const v4u c = {__float_as_uint(r[2]), tag, __float_as_uint(r[3]), tag};
      asm volatile("global_store_dwordx4 %0, %1, %3 sc1\n\tglobal_store_dwordx4 %0, %2, %3 offset:16 sc1\n\ts_nop 1"
                   :: "v"(off), "v"(a), "v"(c), "s"(x3) : "memory");
    }
    STAMP(13);
  } else if (w == 0 && b < 80) {
    const u32 hr = b / 40, rr = b % 40;
    const char* x3 = ws + WS_X3 + (hr * 128 * 320 + 8 * rr) * 8;
    const u32 o0 = (lane >> 2) * 2560 + (lane & 3) * 16;
    v4u g3[8];
    for (u32 spin = 0; spin < SPIN_CAP; spin++) {
      asm volatile("global_load_dwordx4 %0, %8, %16 sc1\n\tglobal_load_dwordx4 %1, %9, %16 sc1\n\t"
                   "global_load_dwordx4 %2, %10, %16 sc1\n\tglobal_load_dwordx4 %3, %11, %16 sc1\n\t"
                   "global_load_dwordx4 %4, %12, %16 sc1\n\tglobal_load_dwordx4 %5, %13, %16 sc1\n\t"
                   "global_load_dwordx4 %6, %14, %16 sc1\n\tglobal_load_dwordx4 %7, %15, %16 sc1\n\ts_waitcnt vmcnt(0)"
                   : "=&v"(g3[0]), "=&v"(g3[1]), "=&v"(g3[2]), "=&v"(g3[3]), "=&v"(g3[4]), "=&v"(g3[5]), "=&v"(g3[6]), "=&v"(g3[7])
                   : "v"(o0), "v"(o0 + 40960), "v"(o0 + 2 * 40960), "v"(o0 + 3 * 40960), "v"(o0 + 4 * 40960),
                     "v"(o0 + 5 * 40960), "v"(o0 + 6 * 40960), "v"(o0 + 7 * 40960), "s"(x3));
      bool ok = true;
#pragma unroll
      for (int e = 0; e < 8; e++) ok = ok && g3[e].y == tag && g3[e].w == tag;
      if (__all(ok)) break;
    }
    STAMP(13);
    float a0 = 0.f, a1 = 0.f;
#pragma unroll
    for (int e = 0; e < 8; e++) { a0 += __uint_as_float(g3[e].x); a1 += __uint_as_float(g3[e].z); }
    a0 += dpp<ROR4>(a0); a1 += dpp<ROR4>(a1);
    a0 += dpp<ROR8>(a0); a1 += dpp<ROR8>(a1);
    a0 = swap32_sum(swap16_sum(a0)); a1 = swap32_sum(swap16_sum(a1));
    if (lane < 4) *(GAS u32*)(out + (8 * b + 2 * lane) * 2) = pk_bf16(a0, a1);
    if (b == 0 && lane == 0) __hip_atomic_store((GAS u32*)(ws + WS_EPOCH), tag, __ATOMIC_RELAXED, __HIP_MEMORY_SCOPE_AGENT);
  }
  STAMP(14);
  if (b == 0 && w == 0) {
    u32 mn = 0xffffffffu, mx = 0;
    const u32 me = (u32)stamp[0], en = (u32)stamp[14];
    for (u32 spin = 0; spin < SPIN_CAP; spin++) {
      bool ok = true; mn = 0xffffffffu; mx = 0;
      for (int e = 0; e < 4; e++) {
        const u64 g = __hip_atomic_load((GAS u64*)(ws + (3u << 20) + (e * 64 + lane) * 8), __ATOMIC_RELAXED, __HIP_MEMORY_SCOPE_AGENT);
        ok = ok && (u32)(g >> 32) == tag;
        const u32 rel = (u32)g - me + 100000u;
        mn = min(mn, rel); mx = max(mx, rel);
      }
      if (__all(ok)) break;
    }
    for (int o = 1; o < 64; o <<= 1) { mn = min(mn, (u32)__shfl_xor((int)mn, o)); mx = max(mx, (u32)__shfl_xor((int)mx, o)); }
    const u64 d = (u64)(100000u - mn) * 20;
    const u64 t0 = __builtin_amdgcn_s_memrealtime();
    while (__builtin_amdgcn_s_memrealtime() - t0 < d) __builtin_amdgcn_s_sleep(1);
  }
}

}

extern "C" void kernel_launch(void* const* d_in, const int* in_sizes, int n_in, void* d_out, int out_size, void* d_ws,
                              size_t ws_size, hipStream_t stream) {
  fused_decode_kernel<<<256, 512, 0, stream>>>(
      (const char*)d_in[0], (const char*)d_in[1], (const char*)d_in[2], (const char*)d_in[3], (const char*)d_in[4],
      (const char*)d_in[6], (const char*)d_in[7], (const char*)d_in[8], (const char*)d_in[9], (char*)d_out, (char*)d_ws);
}
```

```cpp
#include <hip/hip_runtime.h>
#include <hip/hip_bf16.h>
#include <stdint.h>

namespace {

typedef unsigned u32;
typedef unsigned long long u64;
typedef unsigned v4u __attribute__((ext_vector_type(4)));
typedef unsigned v2u __attribute__((ext_vector_type(2)));
typedef float f32x4 __attribute__((ext_vector_type(4)));
typedef float f32x2 __attribute__((ext_vector_type(2)));
typedef short bf16x8 __attribute__((ext_vector_type(8)));
typedef __bf16 bf16x2 __attribute__((ext_vector_type(2)));
#define GAS __attribute__((address_space(1)))
#define LAS __attribute__((address_space(3)))

constexpr int DM = 640, NH = 32, SK = 4096, DH = 192, WIN = 1024, DF = 1536, HD = NH * DH;

constexpr u32 WS_EPOCH = 0;
constexpr u32 WS_TREF = 256;
constexpr u32 WS_X1Q = 4096;
constexpr u32 WS_X1M = WS_X1Q + 32 * 8 * 512;
constexpr u32 WS_X2 = WS_X1M + 8 * 10 * 192 * 8;
constexpr u32 WS_X3 = WS_X2 + 32 * 8 * 1024;
constexpr u32 WS_END = WS_X3 + 2 * 128 * 320 * 8;

constexpr u32 SPIN_CAP = 1u << 16;

constexpr int ND0 = 2;
constexpr int ND1 = 5;
constexpr u32 TG1 = 430;
constexpr u32 TG2 = 690;
constexpr u32 TG3 = 850;
constexpr u32 CODE_BYTES = 10240;

template <int CTRL> __device__ __forceinline__ float dpp(float x) {
  return __builtin_bit_cast(float, __builtin_amdgcn_mov_dpp(__builtin_bit_cast(int, x), CTRL, 0xf, 0xf, true));
}
constexpr int XOR1 = 0xB1, XOR2 = 0x4E, HMIR = 0x141, ROR8 = 0x128, ROR4 = 0x124;
__device__ __forceinline__ float swap16_max(float x) {
  auto s = __builtin_amdgcn_permlane16_swap(__float_as_uint(x), __float_as_uint(x), false, false);
  return fmaxf(__uint_as_float(s[0]), __uint_as_float(s[1]));
}
__device__ __forceinline__ float swap32_max(float x) {
  auto s = __builtin_amdgcn_permlane32_swap(__float_as_uint(x), __float_as_uint(x), false, false);
  return fmaxf(__uint_as_float(s[0]), __uint_as_float(s[1]));
}
__device__ __forceinline__ float swap16_sum(float x) {
  auto s = __builtin_amdgcn_permlane16_swap(__float_as_uint(x), __float_as_uint(x), false, false);
  return __uint_as_float(s[0]) + __uint_as_float(s[1]);
}
__device__ __forceinline__ float swap32_sum(float x) {
  auto s = __builtin_amdgcn_permlane32_swap(__float_as_uint(x), __float_as_uint(x), false, false);
  return __uint_as_float(s[0]) + __uint_as_float(s[1]);
}
__device__ __forceinline__ u32 pk_bf16(float lo, float hi) {
  bf16x2 z = {(__bf16)lo, (__bf16)hi};
  return __builtin_bit_cast(u32, z);
}
__device__ __forceinline__ float dot2(u32 a, u32 b, float c) {
  return __builtin_amdgcn_fdot2_f32_bf16(__builtin_bit_cast(bf16x2, a), __builtin_bit_cast(bf16x2, b), c, false);
}
__device__ __forceinline__ f32x4 mfma(const v4u& a, const v4u& tile, f32x4 acc) {
  return __builtin_amdgcn_mfma_f32_16x16x32_bf16(__builtin_bit_cast(bf16x8, a), __builtin_bit_cast(bf16x8, tile), acc, 0, 0, 0);
}
__device__ __forceinline__ v4u selector(u32 cdup, const v4u& msk) { return v4u{cdup & msk.x, cdup & msk.y, cdup & msk.z, cdup & msk.w}; }
__device__ __forceinline__ f32x2 fold(const f32x4& acc) { return f32x2{acc[0] + dpp<ROR8>(acc[1]), acc[2] + dpp<ROR8>(acc[3])}; }
__device__ __forceinline__ void st_granule4(const char* base, u32 off, const v4u& g) {
  asm volatile("global_store_dwordx4 %0, %1, %2 sc1\n\ts_nop 1" :: "v"(off), "v"(g), "s"(base) : "memory");
}
__device__ __forceinline__ void st_triples(const char* base, u32 off8, u32 lane, float v0, float v1, float v2, u32 t16) {
  const u32 lo = pk_bf16(v0, v1), hi = (pk_bf16(v2, v2) & 0xffffu) | (t16 << 16);
  const u32 nlo = __float_as_uint(dpp<XOR1>(__uint_as_float(lo))), nhi = __float_as_uint(dpp<XOR1>(__uint_as_float(hi)));
  if (!(lane & 1)) st_granule4(base, off8, v4u{lo, hi, nlo, nhi});
}
#define BAR() asm volatile("s_waitcnt lgkmcnt(0)\n\ts_barrier" ::: "memory")
#define LDT(dst, voff, base, imm) asm volatile("global_load_dwordx4 %0, %1, %2 offset:" #imm " nt" : "=v"(dst) : "v"(voff), "s"(base))
#define WAITV(n) asm volatile("s_waitcnt vmcnt(%c0)" :: "i"(n))

struct Lds {
  float paq[2 * 256];
  float pam[2 * 64];
  float po[8 * 256];
  float ml[16];
  u32 cd[64];
  u32 qs[96];
  u32 tag;
  u32 flag;
  float gate;
  u32 pad0_;
  u32 x2cnt;
  u32 x3cnt;
  u32 dump[2];
};
constexpr u32 L_PAQ = 0, L_PAM = 2048, L_PO = L_PAM + 512, L_ML = L_PO + 8192, L_CD = L_ML + 64, L_QS = L_CD + 256, L_TAG = L_QS + 384,
              L_FLAG = L_TAG + 4, L_X2CNT = L_TAG + 16, L_X3CNT = L_TAG + 20, L_DUMP = L_TAG + 24;
__device__ __forceinline__ u32 slot3(u32 c) { const u32 t = (c * 21846) >> 16; return 4 * t + (c - 3 * t); }

__global__ void __launch_bounds__(768, 3)
fused_decode_kernel(const char* x, const char* kc, const char* vc, const char* wq, const char* wo, const char* wg,
                    const char* wu, const char* wd, const char* gw, char* out, char* ws) {
  __shared__ __attribute__((aligned(16))) Lds lds;
  const u32 tid = threadIdx.x, b = blockIdx.x;
  u32 w;
  u64 pc0;
  u32 xs, va, RS, t_a, t_b;
  v4u ta[8];
  const char* abase;
  asm volatile(
      "s_load_dwordx16 s[4:19], %[ka], 0x0\n\t"
      "s_load_dword s20, %[ka], 0x50\n\t"
      "v_lshrrev_b32 %[ta], 6, %[tid]\n\t"
      "s_getpc_b64 s[42:43]\n\t"
      "s_and_b32 s31, %[b], 7\n\t"
      "v_readfirstlane_b32 %[w], %[ta]\n\t"
      "s_lshr_b32 s21, %[b], 5\n\t"
      "s_bfe_u32 s33, %[b], 0x20003\n\t"
      "s_lshl_b32 s34, s31, 2\n\t"
      "s_or_b32 s33, s33, s34\n\t"
      "s_cmp_gt_u32 %[w], 7\n\t"
      "s_cbranch_scc1 2f\n\t"
      "s_and_b32 s34, %[w], 3\n\t"
      "s_lshr_b32 s35, %[w], 2\n\t"
      "s_mul_i32 s29, s21, 80\n\t"
      "s_mul_i32 s36, s35, 40\n\t"
      "s_add_i32 s29, s29, s36\n\t"
      "s_mul_i32 s37, s33, 3\n\t"
      "s_add_i32 s37, s37, s34\n\t"
      "s_movk_i32 s28, 0x3000\n\t"
      "s_mov_b32 s38, 0\n\t"
      "s_cmp_lg_u32 s34, 3\n\t"
      "s_cbranch_scc1 1f\n\t"
      "s_lshr_b32 s36, %[b], 3\n\t"
      "s_min_u32 s36, s36, 29\n\t"
      "s_mul_i32 s39, s36, 43\n\t"
      "s_lshr_b32 s39, s39, 8\n\t"
      "s_mul_i32 s40, s39, 6\n\t"
      "s_sub_i32 s36, s36, s40\n\t"
      "s_cmp_ge_u32 s36, 3\n\t"
      "s_cselect_b32 s38, 3, 0\n\t"
      "s_sub_i32 s36, s36, s38\n\t"
      "s_lshl_b32 s29, s39, 7\n\t"
      "s_lshl_b32 s40, s35, 6\n\t"
      "s_add_i32 s29, s29, s40\n\t"
      "s_mul_i32 s37, s31, 3\n\t"
      "s_add_i32 s37, s37, s36\n\t"
      "s_movk_i32 s28, 0xc00\n"
      "1:\n\t"
      "s_mul_i32 s36, s29, s28\n\t"
      "s_lshl_b32 s37, s37, 7\n\t"
      "s_add_i32 s36, s36, s37\n\t"
      "v_and_b32 %[ta], 63, %[tid]\n\t"
      "v_and_b32 %[tb], 7, %[tid]\n\t"
      "v_lshrrev_b32 %[va], 3, %[ta]\n\t"
      "v_lshlrev_b32 %[tb], 4, %[tb]\n\t"
      "s_lshr_b32 s40, s29, 1\n\t"
      "v_mad_u32_u24 %[va], %[va], s28, %[tb]\n\t"
      "v_add_u32 %[ta], s40, %[ta]\n\t"
      "s_lshl_b32 s39, s28, 3\n\t"
      "v_min_u32 %[ta], 0x13f, %[ta]\n\t"
      "v_add_u32 %[tb], s39, %[va]\n\t"
      "v_lshlrev_b32 %[ta], 2, %[ta]\n\t"
      "s_cmp_eq_u32 s34, 3\n\t"
      "s_waitcnt lgkmcnt(0)\n\t"
      "s_cselect_b32 s26, s14, s10\n\t"
      "s_cselect_b32 s27, s15, s11\n\t"
      "s_cmp_eq_u32 s38, 3\n\t"
      "s_cselect_b32 s26, s16, s26\n\t"
      "s_cselect_b32 s27, s17, s27\n\t"
      "s_add_u32 s26, s26, s36\n\t"
      "s_addc_u32 s27, s27, 0\n\t"
      "global_load_dword %[xs], %[ta], s[4:5]\n\t"
      "global_load_dwordx4 %[a0], %[va], s[26:27] nt\n\t"
      "global_load_dwordx4 %[a1], %[tb], s[26:27] nt\n\t"
      "v_add_u32 %[tb], s39, %[tb]\n\t"
      "s_mul_i32 s40, s39, 3\n\t"
      "global_load_dwordx4 %[a2], %[tb], s[26:27] nt\n\t"
      "s_cmp_lg_u32 s34, 3\n\t"
      "s_cbranch_scc1 2f\n\t"
      "v_add_u32 %[tb], s40, %[tb]\n\t"
      "global_load_dwordx4 %[a5], %[tb], s[26:27] nt\n\t"
      "v_add_u32 %[tb], s39, %[tb]\n\t"
      "global_load_dwordx4 %[a6], %[tb], s[26:27] nt\n\t"
      "v_add_u32 %[tb], s39, %[tb]\n\t"
      "global_load_dwordx4 %[a7], %[tb], s[26:27] nt\n"
      "2:\n\t"
      "s_waitcnt lgkmcnt(0)"
      : "={s[26:27]}"(abase), "={s28}"(RS), [w] "={s41}"(w), "={s[42:43]}"(pc0),
        [xs] "=&v"(xs), [va] "=&v"(va), [ta] "=&v"(t_a), [tb] "=&v"(t_b),
        [a0] "=&v"(ta[0]), [a1] "=&v"(ta[1]), [a2] "=&v"(ta[2]), [a5] "=&v"(ta[5]), [a6] "=&v"(ta[6]), [a7] "=&v"(ta[7])
      : [ka] "s"(__builtin_amdgcn_kernarg_segment_ptr()), [b] "s"(b), [tid] "v"(tid)
      : "scc", "s4", "s5", "s6", "s7", "s8", "s9", "s10", "s11", "s12", "s13", "s14", "s15", "s16", "s17", "s18", "s19", "s20", "s21", "s29", "s31", "s33", "s34", "s35", "s36", "s37", "s38", "s39", "s40");
  const u32 lane = tid & 63;
  const u32 Q = b & 7, i = b >> 3, h = 4 * Q + (i & 3), s = i >> 2, q4 = s >> 1, half = s & 1;
  const u32 kr = lane >> 3, ch = lane & 7, g4 = lane >> 4;
  const u32 lb = (u32)(size_t)(LAS char*)&lds;
#define MLP_UNIT() const u32 im = min(i, 29u), rs = (im * 43) >> 8, lc6 = im - 6 * rs, mat = (lc6 * 43) >> 7, lcx = lc6 - 3 * mat
#define GATE(ticks) do { while ((u32)__builtin_amdgcn_s_memrealtime() - tstart < (ticks)) __builtin_amdgcn_s_sleep(1); } while (0)

  if (__builtin_expect(w < 8, 1)) {
    const u32 lc4 = w & 3;
    const bool isM = lc4 == 3;
    const u32 ts = 8 * RS;
    asm volatile("" : "+s"(kc), "+s"(vc), "+s"(wo), "+s"(wd), "+s"(ws));
    const u32 kq = 4 * (h & 3) + q4;
    const u32 kvoff = (h * SK + (SK - WIN) + 128 * s + 16 * w) * 384;
    const u32 hm = (lane & 2) ? 0xffff0000u : 0x0000ffffu, mi = (lane >> 2) & 3;
    v4u msk = {mi == 0 ? hm : 0u, mi == 1 ? hm : 0u, mi == 2 ? hm : 0u, mi == 3 ? hm : 0u};
    const u32 line = 5 * half + min(w, 4u);
    const char* obase = wo + (DH * h + 48 * q4) * 1280 + line * 128;
    const char* dbase = wd + (DH * Q + 12 * kq) * 1280 + line * 128;
    u32 vd = kr * 1280 + ch * 16, vd7 = (kr & 3) * 1280 + ch * 16 + 10240;
    const char* x3st = ws + WS_X3 + ((half * 128 + h * 4 + q4) * 320 + min(w, 4u) * 64) * 8;
    u32 pubo = (8 * ch + 2 * g4) * 8;
    const u32 c0 = 8 * ch + 2 * g4;
    u32 pa_0 = isM ? lb + L_PAM + ((w >> 2) * 64 + c0) * 4 : lb + L_PAQ + ((w >> 2) * 256 + slot3(64 * lc4 + c0)) * 4;
    u32 pa_1 = isM ? pa_0 + 4 : lb + L_PAQ + ((w >> 2) * 256 + slot3(64 * lc4 + c0 + 1)) * 4;
    u32 po_a = lb + L_PO + (w * 256 + slot3(c0)) * 4, po_b = lb + L_PO + (w * 256 + slot3(c0 + 1)) * 4;
    u32 po_c = lb + L_PO + (w * 256 + slot3(64 + c0)) * 4, po_d = lb + L_PO + (w * 256 + slot3(65 + c0)) * 4;
    u32 po_e = lb + L_PO + (w * 256 + slot3(128 + c0)) * 4, po_f = lb + L_PO + (w * 256 + slot3(129 + c0)) * 4;
    u32 cd_r = lb + L_CD + (2 * g4 + (lane & 1)) * 32;
    u32 qs_r = lb + L_QS + ch * 16;
    u32 vkv = kr * 384 + ch * 16;
    const char* kbase = kc + kvoff;
    const char* vbase = vc + kvoff;
    u32 va3 = va + 3 * ts, va4 = va + 4 * ts;
    asm volatile("" : "+v"(msk), "+v"(vd), "+v"(vd7), "+v"(pa_0), "+v"(pa_1), "+v"(pubo), "+v"(vkv), "+v"(va3), "+v"(va4),
                 "+v"(cd_r), "+v"(qs_r));
    asm volatile("" : "+v"(po_a), "+v"(po_b), "+v"(po_c), "+v"(po_d), "+v"(po_e), "+v"(po_f));
    asm volatile("" : "+s"(obase), "+s"(dbase), "+s"(x3st), "+s"(kbase), "+s"(vbase));
#define WAITM(nm, n) asm volatile("s_waitcnt vmcnt(%c1)\n\ts_cmp_eq_u32 %0, 3\n\ts_cbranch_scc1 1f\n\ts_waitcnt vmcnt(%c2)\n1:" :: "s"(lc4), "i"(nm), "i"(n) : "scc")
    WAITM(5, 2);
    LDT(ta[3], va3, abase, 0);
    asm volatile("" : "+v"(xs));
    u32 xd[8];
    {
      const u32 psel = (lane & 1) ? 0x03020302u : 0x01000100u;
#pragma unroll
      for (int G = 0; G < 8; G++) {
        const u32 d = __builtin_amdgcn_ds_bpermute((4 * G + g4) * 4, xs);
        xd[G] = __builtin_amdgcn_perm(d, d, psel);
      }
    }
    v4u sa[5];
#pragma unroll
    for (int G = 0; G < 5; G++) sa[G] = selector(xd[G], msk);
    WAITM(5, 2);
    LDT(ta[4], va4, abase, 0);
    asm volatile("" : "+v"(sa[0]), "+v"(sa[1]), "+v"(sa[2]), "+v"(sa[3]), "+v"(sa[4]), "+v"(xd[5]), "+v"(xd[6]), "+v"(xd[7]));
    v4u tk[6], tv[6];
    WAITM(4, 2);
    LDT(tk[0], vkv, kbase, 0);
    WAITM(3, 2);
    LDT(tk[1], vkv, kbase, 128);
    WAITM(3, 2);
    LDT(tk[2], vkv, kbase, 256);
    WAITV(3);
    asm volatile("" : "+v"(ta[0]), "+v"(ta[1]), "+v"(ta[2]), "+v"(ta[3]), "+v"(ta[4]), "+v"(ta[5]), "+v"(ta[6]), "+v"(ta[7]));
    {
      f32x4 acc = {0.f, 0.f, 0.f, 0.f};
#pragma unroll
      for (int G = 0; G < 5; G++) acc = mfma(sa[G], ta[G], acc);
      if (isM) {
#pragma unroll
        for (int G = 5; G < 8; G++) acc = mfma(selector(xd[G], msk), ta[G], acc);
      }
      const f32x2 r = fold(acc);
      if (!(lane & 8)) { *(LAS float*)(size_t)pa_0 = r[0]; *(LAS float*)(size_t)pa_1 = r[1]; }
    }
    BAR();
    WAITV(2); LDT(tk[3], vkv, kbase, 3072);
    WAITV(2); LDT(tk[4], vkv, kbase, 3200);
    WAITV(2); LDT(tk[5], vkv, kbase, 3328);
    WAITV(2); LDT(tv[0], vkv, vbase, 0);
    WAITV(2); LDT(tv[1], vkv, vbase, 128);
    WAITV(2); LDT(tv[2], vkv, vbase, 256);
    WAITV(2); LDT(tv[3], vkv, vbase, 3072);
    WAITV(2); LDT(tv[4], vkv, vbase, 3200);
    WAITV(2); LDT(tv[5], vkv, vbase, 3328);
    asm volatile("" : "+v"(tk[0]), "+v"(tk[1]), "+v"(tk[2]), "+v"(tk[3]), "+v"(tk[4]), "+v"(tk[5]));
    BAR();
    f32x4 ao[3];
    v4u td[8];
#define LDT_IF_D(dst, voff, base)                                                                  \
    asm volatile("s_cmp_gt_u32 %3, 4\n\ts_cbranch_scc1 1f\n\tglobal_load_dwordx4 %0, %1, %2 nt\n1:" \
                 : "=v"(dst) : "v"(voff), "s"(base), "s"(w) : "scc")
#pragma unroll
    for (int G = 0; G < ND0; G++) LDT_IF_D(td[G], vd + G * 10240, obase);
    {
      const v4u q0 = *(const LAS v4u*)(size_t)qs_r, q1 = *(const LAS v4u*)(size_t)(qs_r + 128), q2 = *(const LAS v4u*)(size_t)(qs_r + 256);
      float s0 = 0.f, s1 = 0.f, s2 = 0.f, s3 = 0.f;
#pragma unroll
      for (int e = 0; e < 4; e++) { s0 = dot2(tk[0][e], q0[e], s0); s1 = dot2(tk[3][e], q0[e], s1); }
#pragma unroll
      for (int e = 0; e < 4; e++) { s2 = dot2(tk[1][e], q1[e], s2); s3 = dot2(tk[4][e], q1[e], s3); }
#pragma unroll
      for (int e = 0; e < 4; e++) { s0 = dot2(tk[2][e], q2[e], s0); s1 = dot2(tk[5][e], q2[e], s1); }
      s0 += s2; s1 += s3;
      s0 += dpp<XOR1>(s0); s1 += dpp<XOR1>(s1);
      s0 += dpp<XOR2>(s0); s1 += dpp<XOR2>(s1);
      s0 += dpp<HMIR>(s0); s1 += dpp<HMIR>(s1);
      constexpr float C2 = 0.07216878364870322f * 1.4426950408889634f;
      s0 *= C2; s1 *= C2;
      float m = fmaxf(s0, s1);
      m = fmaxf(m, dpp<ROR8>(m));
      m = swap32_max(swap16_max(m));
      if (lane == 0) lds.ml[w] = m;
      BAR();
      {
        const f32x4 m0 = *(const LAS f32x4*)(size_t)(lb + L_ML), m1 = *(const LAS f32x4*)(size_t)(lb + L_ML + 16);
        m = fmaxf(fmaxf(fmaxf(m0.x, m0.y), fmaxf(m0.z, m0.w)), fmaxf(fmaxf(m1.x, m1.y), fmaxf(m1.z, m1.w)));
      }
      const float p0 = __builtin_amdgcn_exp2f(s0 - m), p1 = __builtin_amdgcn_exp2f(s1 - m);
      const u32 pd0 = pk_bf16(p0, p0), pd1 = pk_bf16(p1, p1);
      const bool same = (lane & 1) == ((lane >> 3) & 1);
      const u32 pr0 = __float_as_uint(dpp<ROR8>(__uint_as_float(pd0))), pr1 = __float_as_uint(dpp<ROR8>(__uint_as_float(pd1)));
      v4u a0 = selector(same ? pd0 : pr0, msk);
      asm volatile("s_waitcnt vmcnt(%c5)\n\ts_cmp_le_u32 %4, 4\n\ts_cbranch_scc1 1f\n\ts_waitcnt vmcnt(3)\n1:"
                   : "+v"(tv[0]), "+v"(tv[1]), "+v"(tv[2]), "+v"(a0) : "s"(w), "i"(3 + ND0) : "scc");
      const f32x4 zero = {0.f, 0.f, 0.f, 0.f};
#pragma unroll
      for (int e = 0; e < 3; e++) ao[e] = mfma(a0, tv[e], zero);
      float l = __uint_as_float(pd0 & 0xffff0000u) + __uint_as_float(pd1 & 0xffff0000u);
      l += dpp<ROR8>(l);
      l = swap32_sum(swap16_sum(l));
      if (lane == 0) lds.ml[8 + w] = l;
      v4u a1 = selector(same ? pd1 : pr1, msk);
      asm volatile("" : "+v"(a1), "+v"(ao[0]), "+v"(ao[1]), "+v"(ao[2]));
#pragma unroll
      for (int G = ND0; G < ND1; G++) LDT_IF_D(td[G], vd + G * 10240, obase);
      asm volatile("s_waitcnt vmcnt(%c4)\n\ts_cmp_le_u32 %3, 4\n\ts_cbranch_scc1 1f\n\ts_waitcnt vmcnt(0)\n1:"
                   : "+v"(tv[3]), "+v"(tv[4]), "+v"(tv[5]) : "s"(w), "i"(ND1) : "scc");
#pragma unroll
      for (int e = 0; e < 3; e++) ao[e] = mfma(a1, tv[3 + e], ao[e]);
    }
    {
      const f32x2 r0 = fold(ao[0]), r1 = fold(ao[1]), r2 = fold(ao[2]);
      if (!(lane & 8)) {
        *(LAS float*)(size_t)po_a = r0[0]; *(LAS float*)(size_t)po_b = r0[1];
        *(LAS float*)(size_t)po_c = r1[0]; *(LAS float*)(size_t)po_d = r1[1];
        *(LAS float*)(size_t)po_e = r2[0]; *(LAS float*)(size_t)po_f = r2[1];
      }
    }
    BAR();
    if (__builtin_expect(w <= 4, 1)) {
#pragma unroll
      for (int G = ND1; G < 6; G++) LDT(td[G], vd + G * 10240, obase, 0);
      LDT(td[6], vd, dbase, 0);
      LDT(td[7], vd7, dbase, 0);
      u32 fl_r = lb + L_TAG;
      asm volatile("" : "+v"(fl_r));
      asm volatile("s_waitcnt vmcnt(0)"
                   : "+v"(td[0]), "+v"(td[1]), "+v"(td[2]), "+v"(td[3]), "+v"(td[4]), "+v"(td[5]), "+v"(td[6]), "+v"(td[7]));
      v2u tf;
      v4u c0, c1;
#pragma nounroll
      for (u32 spin = 0; spin < (SPIN_CAP << 4); spin++) {
        asm volatile("ds_read_b64 %0, %3\n\tds_read_b128 %1, %4\n\tds_read_b128 %2, %4 offset:16\n\ts_waitcnt lgkmcnt(0)"
                     : "=&v"(tf), "=&v"(c0), "=&v"(c1) : "v"(fl_r), "v"(cd_r) : "memory");
        if (__builtin_amdgcn_readfirstlane(tf.y)) break;
      }
      f32x4 acc = {0.f, 0.f, 0.f, 0.f};
#pragma unroll
      for (int G = 0; G < 4; G++) acc = mfma(selector(c0[G], msk), td[G], acc);
#pragma unroll
      for (int G = 0; G < 4; G++) acc = mfma(selector(c1[G], msk), td[4 + G], acc);
      const f32x2 r = fold(acc);
      if (!(lane & 8)) st_granule4(x3st, pubo, v4u{__float_as_uint(r[0]), tf.x, __float_as_uint(r[1]), tf.x});
      if (lane == 0) asm volatile("ds_add_u32 %0, %1" :: "v"(fl_r + 20), "v"(1u) : "memory");
    }
  } else {
    u32 tstart = (u32)__builtin_amdgcn_s_memrealtime();
    asm volatile("" : "+s"(tstart));
    const u32 c = w - 8;
    MLP_UNIT();
    const u32 kq = 4 * (h & 3) + q4;
    asm volatile("" : "+s"(x), "+s"(gw), "+s"(out), "+s"(ws));
    u32 ep, pfd;
    v4u xa, xb, ga, gb;
    {
      const u32 z0 = 0, o2 = min(lane, 39u) * 32;
      asm volatile("global_load_dword %0, %1, %2 sc1" : "=v"(ep) : "v"(z0), "s"(ws));
      asm volatile("s_cmp_lg_u32 %7, 3\n\ts_cbranch_scc1 1f\n\t"
                   "global_load_dwordx4 %0, %4, %5\n\tglobal_load_dwordx4 %1, %4, %5 offset:16\n\t"
                   "global_load_dwordx4 %2, %4, %6\n\tglobal_load_dwordx4 %3, %4, %6 offset:16\n1:"
                   : "=&v"(xa), "=&v"(xb), "=&v"(ga), "=&v"(gb) : "v"(o2), "s"(x), "s"(gw), "s"(c) : "scc");
      const char* cb = (const char*)pc0;
      const u32 po = min(c * 4096 + lane * 64, CODE_BYTES - 64);
      asm volatile("s_cmp_ge_u32 %3, %4\n\ts_cbranch_scc1 1f\n\tglobal_load_dword %0, %1, %2\n1:" : "=v"(pfd) : "v"(po), "s"(cb), "s"(i), "i"(2) : "scc");
    }
    __builtin_amdgcn_s_setprio(3);
    asm volatile("s_waitcnt vmcnt(0)" : "+v"(ep), "+v"(xa), "+v"(xb), "+v"(ga), "+v"(gb), "+v"(pfd));
    const u32 tag = __builtin_amdgcn_readfirstlane(ep) + 1u;
    const u32 t16 = tag & 0xffffu;
    if (c < 2) {
      const char* x1base = ws + (c == 0 ? WS_X1Q + (h * 8 + s) * 512 : WS_X1M + (Q * 10 + mat * 5 + rs) * 1536 + lcx * 512);
      const char* x2base = ws + WS_X2 + (h * 8 + s) * 1024;
      u32 lane16 = lane * 16, lane8 = lane * 8;
      u32 a0 = lb + (c == 0 ? L_PAQ + lane * 16 : L_PAM + ((2 * lane) & 63) * 4);
      u32 po_r = lb + L_PO + lane * 16;
      u32 ml_r = lb + L_ML;
      asm volatile("" : "+v"(lane16), "+v"(lane8), "+v"(a0), "+v"(po_r), "+v"(ml_r));
      asm volatile("" : "+s"(x1base), "+s"(x2base));
      if (c == 0 && lane == 0) { lds.tag = tag; lds.flag = 0; lds.x2cnt = 0; lds.x3cnt = 0; }
      if (b == 0 && c == 0 && lane == 0)
        __hip_atomic_store((GAS u64*)(ws + WS_TREF), ((u64)tag << 32) | tstart, __ATOMIC_RELAXED, __HIP_MEMORY_SCOPE_AGENT);
      BAR();
      if (c == 0) {
        f32x4 u, v;
        asm volatile("ds_read_b128 %0, %2\n\tds_read_b128 %1, %2 offset:1024\n\ts_waitcnt lgkmcnt(0)" : "=&v"(u), "=&v"(v) : "v"(a0) : "memory");
        st_triples(x1base, lane8, lane, u[0] + v[0], u[1] + v[1], u[2] + v[2], t16);
      } else {
        f32x2 u, v;
        asm volatile("ds_read_b64 %0, %2\n\tds_read_b64 %1, %2 offset:256\n\ts_waitcnt lgkmcnt(0)" : "=&v"(u), "=&v"(v) : "v"(a0) : "memory");
        if (lane < 32 && i < 30) st_granule4(x1base, lane16, v4u{__float_as_uint(u[0] + v[0]), tag, __float_as_uint(u[1] + v[1]), tag});
      }
      BAR();
      BAR();
      BAR();
      {
        if (c == 0) {
          f32x4 pv[8];
          asm volatile("ds_read_b128 %0, %8\n\tds_read_b128 %1, %8 offset:1024\n\tds_read_b128 %2, %8 offset:2048\n\t"
                       "ds_read_b128 %3, %8 offset:3072\n\tds_read_b128 %4, %8 offset:4096\n\tds_read_b128 %5, %8 offset:5120\n\t"
                       "ds_read_b128 %6, %8 offset:6144\n\tds_read_b128 %7, %8 offset:7168\n\ts_waitcnt lgkmcnt(0)"
                       : "=&v"(pv[0]), "=&v"(pv[1]), "=&v"(pv[2]), "=&v"(pv[3]), "=&v"(pv[4]), "=&v"(pv[5]), "=&v"(pv[6]), "=&v"(pv[7])
                       : "v"(po_r) : "memory");
          const f32x4 v = ((pv[0] + pv[1]) + (pv[2] + pv[3])) + ((pv[4] + pv[5]) + (pv[6] + pv[7]));
          st_triples(x2base, lane8, lane, v[0], v[1], v[2], t16);
        } else {
          f32x4 m0, m1, l0, l1;
          asm volatile("ds_read_b128 %0, %4\n\tds_read_b128 %1, %4 offset:16\n\tds_read_b128 %2, %4 offset:32\n\t"
                       "ds_read_b128 %3, %4 offset:48\n\ts_waitcnt lgkmcnt(0)"
                       : "=&v"(m0), "=&v"(m1), "=&v"(l0), "=&v"(l1) : "v"(ml_r) : "memory");
          const float M = fmaxf(fmaxf(fmaxf(m0.x, m0.y), fmaxf(m0.z, m0.w)), fmaxf(fmaxf(m1.x, m1.y), fmaxf(m1.z, m1.w)));
          const float L = ((l0.x + l0.y) + (l0.z + l0.w)) + ((l1.x + l1.y) + (l1.z + l1.w));
          if (lane == 0) st_granule4(x2base + 512, lane16, v4u{__float_as_uint(M), tag, __float_as_uint(L), tag});
        }
        if (lane == 0) asm volatile("ds_add_u32 %0, %1" :: "v"(lb + L_X2CNT), "v"(1u) : "memory");
      }
    } else {
      const u32 hm = (lane & 2) ? 0xffff0000u : 0x0000ffffu, mi = (lane >> 2) & 3;
      v4u msk = {mi == 0 ? hm : 0u, mi == 1 ? hm : 0u, mi == 2 ? hm : 0u, mi == 3 ? hm : 0u};
      const u32 mcol = 12 * kq + 2 * min(ch, 5u);
      const char* g1base = ws + WS_X1Q + h * (8 * 512);
      const char* gmbase = ws + WS_X1M + Q * (10 * 1536);
      u32 g1a = kr * 512 + ch * 16;
      u32 gma = min(kr, 4u) * 1536 + mcol * 8, gmb = gma + 5 * 1536;
      const char* g2base = ws + WS_X2 + h * (8 * 1024);
      u32 go = kr * 1024 + (8 * q4 + ch) * 16;
      u32 gm = (2 * g4 + (lane & 1)) * 1024 + 512;
      const u32 e0 = 6 * ch + (g4 == 0 ? 0u : (g4 == 1 ? 2u : (g4 == 2 ? 3u : 5u)));
      const bool two = !(g4 & 1), fl = !(lane & 8);
      u32 qs_w0 = fl ? lb + L_QS + 2 * e0 : lb + L_DUMP, qs_w1 = fl && two ? lb + L_QS + 2 * e0 + 2 : lb + L_DUMP;
      u32 cd_w0 = fl ? lb + L_CD + (e0 & 7) * 32 + (e0 >> 3) * 4 : lb + L_DUMP;
      u32 cd_w1 = fl && two ? lb + L_CD + ((e0 + 1) & 7) * 32 + ((e0 + 1) >> 3) * 4 : lb + L_DUMP;
      const u32 hr = b / 40, rr = b % 40;
      const char* x3 = ws + WS_X3 + (hr * 128 * 320 + 8 * rr) * 8;
      u32 o0 = (lane >> 2) * 2560 + (lane & 3) * 16;
      asm volatile("" : "+v"(g1a), "+v"(gma), "+v"(gmb), "+v"(msk), "+v"(qs_w0), "+v"(qs_w1));
      asm volatile("" : "+v"(go), "+v"(gm), "+v"(cd_w0), "+v"(cd_w1), "+v"(o0));
      asm volatile("" : "+s"(g1base), "+s"(gmbase), "+s"(g2base), "+s"(x3));
      float gate = 0.f;
      if (c == 3) {
        float z = 0.f;
#pragma unroll
        for (int e = 0; e < 4; e++) z = dot2(xb[e], gb[e], dot2(xa[e], ga[e], z));
        z = lane < 40 ? z : 0.f;
        z += dpp<XOR1>(z); z += dpp<XOR2>(z); z += dpp<HMIR>(z); z += dpp<ROR8>(z);
        z = swap32_sum(swap16_sum(z));
        gate = __builtin_amdgcn_rcpf(1.f + __builtin_amdgcn_exp2f(-1.4426950408889634f * z));
        if (lane == 0) lds.gate = gate;
      }
      BAR();
      gate = lds.gate;
      asm volatile("" : "+v"(gate));
      v4u g1[4], gg, gu;
      if (c == 2) {
        __builtin_amdgcn_s_sleep(20);
        GATE(TG1);
#pragma nounroll
        for (u32 spin = 0; spin < SPIN_CAP; spin++) {
          asm volatile("global_load_dwordx4 %0, %6, %9 sc1\n\tglobal_load_dwordx4 %1, %6, %9 offset:128 sc1\n\t"
                       "global_load_dwordx4 %2, %6, %9 offset:256 sc1\n\tglobal_load_dwordx4 %3, %6, %9 offset:384 sc1\n\t"
                       "global_load_dwordx4 %4, %7, %10 sc1\n\tglobal_load_dwordx4 %5, %8, %10 sc1\n\t"
                       "s_waitcnt vmcnt(0)"
                       : "=&v"(g1[0]), "=&v"(g1[1]), "=&v"(g1[2]), "=&v"(g1[3]), "=&v"(gg), "=&v"(gu)
                       : "v"(g1a), "v"(gma), "v"(gmb), "s"(g1base), "s"(gmbase));
          u32 bad = 0;
#pragma unroll
          for (int e = 0; e < 4; e++) bad |= ((g1[e].y >> 16) ^ t16) | ((g1[e].w >> 16) ^ t16);
          if (__all(bad == 0)) break;
        }
        const v4u one = selector(0x3f803f80u, msk);
        const f32x4 zero = {0.f, 0.f, 0.f, 0.f};
#pragma unroll
        for (int e = 0; e < 4; e++) {
          const v4u t = {g1[e].x, g1[e].y & 0xffffu, g1[e].z, g1[e].w & 0xffffu};
          const f32x2 r = fold(mfma(one, t, zero));
          const u32 pk = pk_bf16(r[0], r[1]);
          *(LAS unsigned short*)(size_t)(qs_w0 + (fl ? 96 * e : 0)) = (unsigned short)pk;
          *(LAS unsigned short*)(size_t)(qs_w1 + (fl && two ? 96 * e : 0)) = (unsigned short)(pk >> 16);
        }
        BAR();
        BAR();
#pragma nounroll
        for (u32 spin = 0; spin < SPIN_CAP; spin++) {
          if (__all(gg.y == tag && gg.w == tag && gu.y == tag && gu.w == tag)) break;
          asm volatile("global_load_dwordx4 %0, %2, %4 sc1\n\tglobal_load_dwordx4 %1, %3, %4 sc1\n\ts_waitcnt vmcnt(0)"
                       : "=&v"(gg), "=&v"(gu) : "v"(gma), "v"(gmb), "s"(gmbase));
        }
        const bool live = kr < 5;
        float g0 = live ? __uint_as_float(gg.x) : 0.f, g1v = live ? __uint_as_float(gg.z) : 0.f;
        float u0 = live ? __uint_as_float(gu.x) : 0.f, u1 = live ? __uint_as_float(gu.z) : 0.f;
        g0 += dpp<ROR8>(g0); g1v += dpp<ROR8>(g1v); u0 += dpp<ROR8>(u0); u1 += dpp<ROR8>(u1);
        g0 = swap32_sum(swap16_sum(g0)); g1v = swap32_sum(swap16_sum(g1v));
        u0 = swap32_sum(swap16_sum(u0)); u1 = swap32_sum(swap16_sum(u1));
        const float g = (kr & 1) ? g1v : g0, u = (kr & 1) ? u1 : u0;
        const float y2 = 2.f * 0.7978845608028654f * 1.4426950408889634f * (g + 0.044715f * g * g * g);
        const float ge = g - g * __builtin_amdgcn_rcpf(1.f + __builtin_amdgcn_exp2f(y2));
        const float hv = ch < 6 ? ge * u * (1.f - gate) : 0.f;
        const u32 row = 2 * ch + (kr & 1);
        if (kr < 2) lds.cd[(row & 7) * 8 + 6 + (row >> 3)] = pk_bf16(hv, hv);
      } else {
        BAR();
        BAR();
      }
      BAR();
      if (c == 3) {
        v4u g2, gl;
        bool got = false;
#pragma nounroll
        for (u32 spin = 0; spin < (SPIN_CAP << 4); spin++) {
          if (__builtin_amdgcn_readfirstlane(*(volatile LAS u32*)(size_t)(lb + L_X2CNT)) >= 2) break;
          __builtin_amdgcn_s_sleep(1);
        }
        GATE(TG2);
#pragma nounroll
        for (u32 spin = 0; spin < SPIN_CAP; spin++) {
          asm volatile("global_load_dwordx4 %1, %3, %4 sc1\n\tglobal_load_dwordx4 %0, %2, %4 sc1\n\ts_waitcnt vmcnt(0)"
                       : "=&v"(g2), "=&v"(gl) : "v"(go), "v"(gm), "s"(g2base));
          if (__all((g2.y >> 16) == t16 && (g2.w >> 16) == t16 && gl.y == tag && gl.w == tag)) { got = true; break; }
        }
        if (got) {
          const float ms = __uint_as_float(gl.x);
          float M = fmaxf(ms, dpp<XOR1>(ms));
          M = swap32_max(swap16_max(M));
          const float fs = __builtin_amdgcn_exp2f(ms - M);
          float Lp = fs * __uint_as_float(gl.z);
          Lp += dpp<XOR1>(Lp);
          Lp = swap32_sum(swap16_sum(Lp));
          const float cs = fs * gate * __builtin_amdgcn_rcpf(Lp);
          const u32 chi = pk_bf16(cs, cs);
          const float clo = cs - __uint_as_float(chi & 0xffff0000u);
          const v4u t = {g2.x, g2.y & 0xffffu, g2.z, g2.w & 0xffffu};
          const f32x4 zero = {0.f, 0.f, 0.f, 0.f};
          const f32x2 r = fold(mfma(selector(pk_bf16(clo, clo), msk), t, mfma(selector(chi, msk), t, zero)));
          *(volatile LAS u32*)(size_t)cd_w0 = pk_bf16(r[0], r[0]);
          *(volatile LAS u32*)(size_t)cd_w1 = pk_bf16(r[1], r[1]);
          if (lane == 0) *(volatile LAS u32*)(size_t)(lb + L_FLAG) = 1u;
        }
      }
      if (c == 3 && b < 80) {
        v4u g3[8];
        {
          v2u tr;
          const u32 z0 = WS_TREF;
          asm volatile("global_load_dwordx2 %0, %1, %2 sc1\n\ts_waitcnt vmcnt(0)" : "=v"(tr) : "v"(z0), "s"(ws));
          const u32 tt = __builtin_amdgcn_readfirstlane(tr.y), tv0 = __builtin_amdgcn_readfirstlane(tr.x);
          tstart = tt == tag ? tv0 : tstart;
        }
#pragma nounroll
        for (u32 spin = 0; spin < (SPIN_CAP << 4); spin++) {
          if (__builtin_amdgcn_readfirstlane(*(volatile LAS u32*)(size_t)(lb + L_X3CNT)) >= 5) break;
          __builtin_amdgcn_s_sleep(1);
        }
        GATE(TG3);
#pragma nounroll
        for (u32 spin = 0; spin < SPIN_CAP; spin++) {
          asm volatile("global_load_dwordx4 %0, %8, %16 sc1\n\tglobal_load_dwordx4 %1, %9, %16 sc1\n\t"
                       "global_load_dwordx4 %2, %10, %16 sc1\n\tglobal_load_dwordx4 %3, %11, %16 sc1\n\t"
                       "global_load_dwordx4 %4, %12, %16 sc1\n\tglobal_load_dwordx4 %5, %13, %16 sc1\n\t"
                       "global_load_dwordx4 %6, %14, %16 sc1\n\tglobal_load_dwordx4 %7, %15, %16 sc1\n\ts_waitcnt vmcnt(0)"
                       : "=&v"(g3[0]), "=&v"(g3[1]), "=&v"(g3[2]), "=&v"(g3[3]), "=&v"(g3[4]), "=&v"(g3[5]), "=&v"(g3[6]), "=&v"(g3[7])
                       : "v"(o0), "v"(o0 + 40960), "v"(o0 + 2 * 40960), "v"(o0 + 3 * 40960), "v"(o0 + 4 * 40960),
                         "v"(o0 + 5 * 40960), "v"(o0 + 6 * 40960), "v"(o0 + 7 * 40960), "s"(x3));
          u32 mn = 0xffffffffu, mx = 0;
#pragma unroll
          for (int e = 0; e < 8; e++) { mn = min(mn, min(g3[e].y, g3[e].w)); mx = max(mx, max(g3[e].y, g3[e].w)); }
          if (__all(mn == tag && mx == tag)) break;
        }
        float a0 = 0.f, a1 = 0.f;
#pragma unroll
        for (int e = 0; e < 8; e++) { a0 += __uint_as_float(g3[e].x); a1 += __uint_as_float(g3[e].z); }
        a0 += dpp<ROR4>(a0); a1 += dpp<ROR4>(a1);
        a0 += dpp<ROR8>(a0); a1 += dpp<ROR8>(a1);
        a0 = swap32_sum(swap16_sum(a0)); a1 = swap32_sum(swap16_sum(a1));
        if (lane < 4) *(GAS u32*)(out + (8 * b + 2 * lane) * 2) = pk_bf16(a0, a1);
        if (b == 0 && lane == 0) __hip_atomic_store((GAS u32*)(ws + WS_EPOCH), tag, __ATOMIC_RELAXED, __HIP_MEMORY_SCOPE_AGENT);
      }
    }
  }
}

}

extern "C" void kernel_launch(void* const* d_in, const int* in_sizes, int n_in, void* d_out, int out_size, void* d_ws,
                              size_t ws_size, hipStream_t stream) {
  if (n_in < 10 || out_size != DM || ws_size < WS_END) return;
  fused_decode_kernel<<<256, 768, 0, stream>>>(
      (const char*)d_in[0], (const char*)d_in[1], (const char*)d_in[2], (const char*)d_in[3], (const char*)d_in[4],
      (const char*)d_in[6], (const char*)d_in[7], (const char*)d_in[8], (const char*)d_in[9], (char*)d_out, (char*)d_ws);
}
```
